```python
import torch

def _softmax(x, axis=-1):
    return torch.softmax(x, dim=axis)
def _gelu(x):
    return torch.nn.functional.gelu(x, approximate="tanh")
def _silu(x):
    return torch.nn.functional.silu(x)
def _sigmoid(x):
    return torch.sigmoid(x)
def _layernorm(x, axis=-1, eps=1e-5):
    m = x.mean(dim=axis, keepdim=True)
    v = x.var(dim=axis, keepdim=True, unbiased=False)
    return (x - m) / torch.sqrt(v + eps)
def _scatter_add(tgt, idx, src, axis=0):
    return tgt.clone().index_add_(axis, idx, src)
def _segment_sum(src, seg, n):
    out = torch.zeros((n,) + src.shape[1:], dtype=src.dtype)
    return out.index_add_(0, seg, src)
def _pad(x, pw):
    flat = tuple(v for pair in reversed(pw) for v in pair)
    return torch.nn.functional.pad(x, flat)

kernel_name = 'recurrent_scan__mha_block__glu_gated_geglu__prompt'

def setup_inputs():

    _w = (lambda a: (lambda t: t.to(torch.bfloat16) if t.is_floating_point() else t)(torch.as_tensor(a)))
    def _A_setup():
        B, S, D, H = 1, 2048, 512, 32
        return {
            "x": _w((lambda *s: torch.randn(*s, dtype=torch.bfloat16) * 0.05)(B, S, D)),
            "wqkv": _w((lambda *s: torch.randn(*s, dtype=torch.bfloat16) * 0.2)(D, 3 * D)),
            "wo": _w((lambda *s: torch.randn(*s, dtype=torch.bfloat16) * 0.2)(D, D)),
        }
    _A_in = _A_setup()

    def _B_setup():
        B, S, Dm, Df = 1, 2048, 512, 1792
        return {
            "x": _w((lambda *s: torch.randn(*s, dtype=torch.bfloat16) * 0.05)(B, S, Dm)),
            "w_gate": _w((lambda *s: torch.randn(*s, dtype=torch.bfloat16) * 0.1)(Dm, Df) * (Dm ** -0.5)),
            "w_up": _w((lambda *s: torch.randn(*s, dtype=torch.bfloat16) * 0.1)(Dm, Df) * (Dm ** -0.5)),
            "w_down": _w((lambda *s: torch.randn(*s, dtype=torch.bfloat16) * 0.1)(Df, Dm) * (Df ** -0.5)),
        }
    _B_in = _B_setup()

    return {
        "A_x": _A_in["x"],
        "A_wqkv": _A_in["wqkv"],
        "A_wo": _A_in["wo"],
        "B_x": _B_in["x"],
        "B_w_gate": _B_in["w_gate"],
        "B_w_up": _B_in["w_up"],
        "B_w_down": _B_in["w_down"],
    }

def reference(A_x, A_wqkv, A_wo, B_x, B_w_gate, B_w_up, B_w_down):
    def _A_ref(x, wqkv, wo):
        B, S, D = x.shape
        H = 32
        Dh = D // H
        qkv = (lambda x, *d: x.permute(*d))((x @ wqkv).reshape(B, S, 3, H, Dh), 2, 0, 3, 1, 4)
        q, k, v = qkv[0], qkv[1], qkv[2]
        scores = torch.einsum("bhqd,bhkd->bhqk", q, k) * (Dh ** -0.5)
        p = _softmax(scores, axis=-1)
        o = torch.einsum("bhqk,bhkd->bhqd", p, v)
        return (lambda x, *d: x.permute(*d))(o, 0, 2, 1, 3).reshape(B, S, D) @ wo

    def _B_ref(x, w_gate, w_up, w_down):
        g = torch.einsum("bsd,df->bsf", x, w_gate)
        u = torch.einsum("bsd,df->bsf", x, w_up)
        h = _gelu(g) * u
        return torch.einsum("bsf,fd->bsd", h, w_down)

    x = A_x
    xn = _layernorm(x, axis=-1)
    s = _A_ref(xn, A_wqkv, A_wo)
    g = _silu(_B_ref(xn, B_w_gate, B_w_up, B_w_down))
    return x + s * g
```

```cpp
#include <hip/hip_runtime.h>
#include <hip/hip_bf16.h>


typedef float    f32x16 __attribute__((ext_vector_type(16)));
typedef float    f32x4  __attribute__((ext_vector_type(4)));
typedef float    f32x2  __attribute__((ext_vector_type(2)));
typedef __bf16   bf16x8 __attribute__((ext_vector_type(8)));
typedef __bf16   bf16x2 __attribute__((ext_vector_type(2)));
typedef short    s16x4  __attribute__((ext_vector_type(4)));
typedef unsigned u32x4  __attribute__((ext_vector_type(4)));
typedef unsigned u32x2  __attribute__((ext_vector_type(2)));
#define DI __device__ __forceinline__
#define LDS(T) __attribute__((address_space(3))) T

constexpr int S_ = 2048, D_ = 512, H_ = 32, DF_ = 1792;
constexpr size_t WS_Q  = 0;
constexpr size_t WS_KV = 2u << 20;
constexpr size_t WS_XN = 6u << 20;
constexpr size_t WS_O  = 8u << 20;
constexpr size_t WS_H  = 10u << 20;
constexpr size_t WS_WD = 22u << 20;
constexpr size_t WS_WO = 24u << 20;
constexpr size_t WS_CNT = 20u << 20;

DI unsigned cvtpk(float lo, float hi) { f32x2 v = {lo, hi}; return __builtin_bit_cast(unsigned, __builtin_convertvector(v, bf16x2)); }
DI float bf_lo(unsigned u) { return __builtin_bit_cast(float, u << 16); }
DI float bf_hi(unsigned u) { return __builtin_bit_cast(float, u & 0xffff0000u); }
DI float bf_round(float f) { return bf_lo(cvtpk(f, f)); }
DI f32x16 mfma32(u32x4 a, u32x4 b, f32x16 c) {
  return __builtin_amdgcn_mfma_f32_32x32x16_bf16(__builtin_bit_cast(bf16x8, a), __builtin_bit_cast(bf16x8, b), c, 0, 0, 0);
}
DI float dot2(unsigned a, unsigned b, float c) {
  return __builtin_amdgcn_fdot2_f32_bf16(__builtin_bit_cast(bf16x2, a), __builtin_bit_cast(bf16x2, b), c, false);
}
DI u32x2 tr_read(unsigned addr) {
  return __builtin_bit_cast(u32x2, __builtin_amdgcn_ds_read_tr16_b64_v4i16((LDS(s16x4)*)addr));
}
DI u32x4 lds_read128(unsigned addr) { return *(LDS(u32x4)*)addr; }
DI unsigned rfl(unsigned v) { return __builtin_amdgcn_readfirstlane(v); }
DI void dma16(unsigned ldsaddr, const void* gbase, unsigned voff) {
  asm volatile("s_mov_b32 m0, %0\n\ts_nop 0\n\tglobal_load_lds_dwordx4 %1, %2" :: "s"(ldsaddr), "v"(voff), "s"(gbase) : "memory");
}
DI void store_sc1(char* p, const u32x4& v) {
  asm volatile("global_store_dwordx4 %0, %1, off sc1\n\ts_nop 1" :: "v"(p), "v"(v) : "memory");
}
#define WAIT_VM_BARRIER(n) asm volatile("s_waitcnt vmcnt(" #n ")\n\ts_barrier" ::: "memory")

constexpr int K1_LDS = 131072 + 8192;
constexpr unsigned K1_PART = 131072;
constexpr int K1_CODE_KB = 46;
constexpr unsigned K1_DUMMY = 131072 + 7168;
constexpr unsigned K1_FIN = 131072 + 5120;

struct K1Frag { u32x4 x; u32x4 w[3]; };
struct K1Addr { unsigned x[2][4]; unsigned w[2]; };

#define SCHED_FENCE() __builtin_amdgcn_sched_barrier(0)
DI void mfma_acc(f32x16& acc, const u32x4& a, const u32x4& b) {
  asm volatile("v_mfma_f32_32x32x16_bf16 %0, %1, %2, %0" : "+v"(acc) : "v"(a), "v"(b));
}

template <int SLOT, int J>
DI void k1_read(K1Frag& f, const K1Addr& a) {
  constexpr unsigned off = (SLOT & 1) * 32768u;
  f.x = lds_read128(a.x[SLOT >> 1][J] + off);
#pragma unroll
  for (int b = 0; b < 3; ++b) {
    u32x2 lo = tr_read(a.w[SLOT >> 1] + off + 8192u + b * 8192u + J * 2048u);
    u32x2 hi = tr_read(a.w[SLOT >> 1] + off + 8192u + b * 8192u + J * 2048u + 512u);
    f.w[b] = u32x4{lo.x, lo.y, hi.x, hi.y};
  }
}

DI void mfma16_acc(f32x4& acc, const u32x4& a, const u32x4& b) {
  asm volatile("v_mfma_f32_16x16x32_bf16 %0, %1, %2, %0" : "+v"(acc) : "v"(a), "v"(b));
}

DI void sumsq(float& q0, float& q1, unsigned x) {
  unsigned t0, t1;
  asm volatile("v_lshlrev_b32 %2, 16, %4\n\tv_and_b32 %3, 0xffff0000, %4\n\tv_fmac_f32 %0, %2, %2\n\tv_fmac_f32 %1, %3, %3"
               : "+v"(q0), "+v"(q1), "=&v"(t0), "=&v"(t1) : "v"(x));
}

struct K1Sums { f32x4 cs[3]; f32x4 sx; float q0, q1; };

template <int SLOT, int J, int B>
DI u32x4 k1_read_w(const K1Addr& a) {
  constexpr unsigned off = (SLOT & 1) * 32768u + 8192u + B * 8192u + J * 2048u;
  const u32x2 lo = tr_read(a.w[SLOT >> 1] + off), hi = tr_read(a.w[SLOT >> 1] + off + 512u);
  return u32x4{lo.x, lo.y, hi.x, hi.y};
}

template <int SLOT, int J>
DI u32x4 k1_read_x(const K1Addr& a) { return lds_read128(a.x[SLOT >> 1][J] + (SLOT & 1) * 32768u); }

template <int MH, int HP, int J, class R0, class R1, class R2>
DI void k1_step(const K1Frag& cur, f32x16 (&acc)[3], K1Sums& S, const u32x4& sel, R0 r0, R1 r1, R2 r2) {
  mfma_acc(acc[0], cur.w[0], cur.x);
  SCHED_FENCE();
  r0();
  sumsq(S.q0, S.q1, cur.x[2 * HP]);
  SCHED_FENCE();
  if constexpr (((J + MH) & 1) == 0) mfma16_acc(S.cs[0], sel, cur.w[0]);
  mfma_acc(acc[1], cur.w[1], cur.x);
  SCHED_FENCE();
  r1();
  sumsq(S.q0, S.q1, cur.x[2 * HP + 1]);
  SCHED_FENCE();
  if constexpr (((J + MH) & 1) == 1) mfma16_acc(S.cs[1], sel, cur.w[1]);
  mfma_acc(acc[2], cur.x, cur.w[2]);
  SCHED_FENCE();
  r2();
  SCHED_FENCE();
  if constexpr (((J + MH) & 1) == 0) mfma16_acc(S.cs[2], sel, cur.w[2]);
  if constexpr (((J + HP) & 1) == 0) mfma16_acc(S.sx, sel, cur.x);
  SCHED_FENCE();
}

template <int MH, int HP, int SLOT>
DI void k1_chunk(K1Frag (&f)[4], const K1Addr& a, f32x16 (&acc)[3], K1Sums& S, const u32x4& sel) {
  constexpr int N = (SLOT + 1) & 3;
  k1_step<MH, HP, 0>(f[0], acc, S, sel,
      [&] { f[2].x = k1_read_x<SLOT, 2>(a); f[2].w[0] = k1_read_w<SLOT, 2, 0>(a); },
      [&] { f[2].w[1] = k1_read_w<SLOT, 2, 1>(a); f[3].x = k1_read_x<SLOT, 3>(a); },
      [&] { f[2].w[2] = k1_read_w<SLOT, 2, 2>(a); f[3].w[0] = k1_read_w<SLOT, 3, 0>(a); });
  k1_step<MH, HP, 1>(f[1], acc, S, sel,
      [&] { f[3].w[1] = k1_read_w<SLOT, 3, 1>(a); f[3].w[2] = k1_read_w<SLOT, 3, 2>(a); }, [] {}, [] {});
  asm volatile("s_waitcnt lgkmcnt(0)\n\ts_barrier" ::: "memory");
  k1_step<MH, HP, 2>(f[2], acc, S, sel,
      [&] { f[0].x = k1_read_x<N, 0>(a); f[0].w[0] = k1_read_w<N, 0, 0>(a); },
      [&] { f[0].w[1] = k1_read_w<N, 0, 1>(a); }, [&] { f[0].w[2] = k1_read_w<N, 0, 2>(a); });
  k1_step<MH, HP, 3>(f[3], acc, S, sel,
      [&] { f[1].x = k1_read_x<N, 1>(a); f[1].w[0] = k1_read_w<N, 1, 0>(a); },
      [&] { f[1].w[1] = k1_read_w<N, 1, 1>(a); }, [&] { f[1].w[2] = k1_read_w<N, 1, 2>(a); });
}

template <int MH, int HP>
DI void k1_main(const K1Addr& a, f32x16 (&acc)[3], K1Sums& S, const u32x4& sel) {
  K1Frag f[4];
  asm volatile("s_barrier" ::: "memory");
  k1_read<0, 0>(f[0], a);
  k1_read<0, 1>(f[1], a);
#pragma unroll 1
  for (int it = 0; it < 2; ++it) {
    k1_chunk<MH, HP, 0>(f, a, acc, S, sel);
    k1_chunk<MH, HP, 1>(f, a, acc, S, sel);
    k1_chunk<MH, HP, 2>(f, a, acc, S, sel);
    k1_chunk<MH, HP, 3>(f, a, acc, S, sel);
  }
}

extern "C" __global__ void __launch_bounds__(512, 2)
k1_qkv(const __hip_bfloat16* __restrict__ x, const __hip_bfloat16* __restrict__ wqkv, const __hip_bfloat16* __restrict__ wg2,
       const __hip_bfloat16* __restrict__ wu2, const __hip_bfloat16* __restrict__ wd2, const __hip_bfloat16* __restrict__ wo2,
       char* __restrict__ wsp) {
  extern __shared__ __attribute__((aligned(1024))) char smem[];
  const unsigned lds0 = (unsigned)(size_t)(LDS(char)*)smem;
  const int tid = threadIdx.x, lane = tid & 63, wv = rfl(tid >> 6), w = wv & 3;
  const int b = blockIdx.x, xcd = b & 7, idx = b >> 3;
  if (wv < 2) {
    const char* code;
    asm volatile("s_getpc_b64 s[40:41]\n\ts_add_u32 s40, s40, k1_qkv@rel32@lo+4\n\ts_addc_u32 s41, s41, k1_qkv@rel32@hi+12\n\ts_mov_b64 %0, s[40:41]"
                 : "=s"(code) :: "scc", "s40", "s41");
    const int piece = idx + 32 * wv;
    dma16(lds0 + K1_DUMMY, code + (size_t)(piece < K1_CODE_KB ? piece : 0) * 1024, lane * 16);
  }
  const int mi = (xcd & 3) * 8 + (idx & 7);
  const int nj = (xcd >> 2) * 4 + (idx >> 3);
  const int mh = w & 1, hp = w >> 1;
  const int h = lane >> 5, l31 = lane & 31;
  const int mblock = mi * 2 + mh;
  LDS(float)* part = (LDS(float)*)(smem + K1_PART);
  LDS(float)* fin = (LDS(float)*)(smem + K1_FIN);
  const unsigned xrow = mh * 32 + l31;
  const unsigned xsw = (xrow >> 1) & 7;

  const char* xg = (const char*)x + (size_t)mi * 64 * 1024;
  const char* wg = (const char*)wqkv + nj * 128;
  unsigned vo[8];
  auto issue = [&](int c) {
    const unsigned slot = lds0 + (c & 3) * 32768u + w * 2048u;
    const char* xs = xg + c * 128;
    const char* ws = wg + (size_t)c * 64 * 3072;
    dma16(slot, xs, vo[0]);
    dma16(slot + 1024u, xs, vo[1]);
#pragma unroll
    for (int i = 0; i < 6; ++i) dma16(slot + 8192u + (i >> 1) * 8192u + (i & 1) * 1024u, ws, vo[2 + i]);
  };
  {
    const unsigned r0 = w * 16 + (lane >> 3);
    vo[0] = r0 * 1024 + (((lane & 7) ^ ((r0 >> 1) & 7)) << 4);
    vo[1] = (r0 + 8) * 1024 + (((lane & 7) ^ (((r0 + 8) >> 1) & 7)) << 4);
    const unsigned wvo = r0 * 3072 + (((lane & 7) ^ (((r0 >> 1) & 1) << 2)) << 4);
#pragma unroll
    for (int i = 0; i < 6; ++i) vo[2 + i] = wvo + (i >> 1) * 1024 + (i & 1) * 8 * 3072;
  }
  if (wv >= 4) { issue(0); issue(1); }
  int role = wv;
  asm volatile("" : "+s"(role));
  if (role >= 4) {
    asm volatile("" :: "s"(wg2), "s"(wu2), "s"(wd2), "s"(wo2));
    u32x4 keep[2] = {u32x4{0, 0, 0, 0}, u32x4{0, 0, 0, 0}};
    auto grab = [&](int c) {
      if (c == nj) {
        const unsigned base = lds0 + (c & 3) * 32768u + xrow * 128;
        keep[0] = lds_read128(base + (((4 * hp + h) ^ xsw) << 4));
        keep[1] = lds_read128(base + (((4 * hp + 2 + h) ^ xsw) << 4));
        asm volatile("s_waitcnt lgkmcnt(0)" : "+v"(keep[0]), "+v"(keep[1]) :: "memory");
      }
    };
    if (b == 0 && tid < 256 + 48) *(unsigned*)(wsp + WS_CNT + (tid - 256) * 256) = 0u;

    WAIT_VM_BARRIER(8); issue(2); issue(3);
    const int gw = b * 4 + w;
    u32x4 rp[3];
    char* rdst[3];
    {
      const unsigned row = lane >> 3, dv = row * 1024 + (((lane & 7) ^ (((row >> 1) & 1) << 2)) << 4);
      const char* rsrc[3];
      const int pa = gw, pb = gw + 1024, pc = (gw - 768) * 2;
      auto wd_src = [&](int p) { const int t = p / 224, r = p - t * 224; return (const char*)wd2 + (size_t)r * 8192 + t * 128; };
      auto wo_src = [&](int p) { return (const char*)wo2 + (size_t)(p & 63) * 8192 + (p >> 6) * 128; };
      rsrc[0] = wd_src(pa); rdst[0] = wsp + WS_WD + (size_t)pa * 1024;
      if (gw < 768) { rsrc[1] = rsrc[2] = wd_src(pb); rdst[1] = rdst[2] = wsp + WS_WD + (size_t)pb * 1024; }
      else { rsrc[1] = wo_src(pc); rsrc[2] = wo_src(pc + 1); rdst[1] = wsp + WS_WO + (size_t)pc * 1024; rdst[2] = rdst[1] + 1024; }
#pragma unroll
      for (int i = 0; i < 3; ++i)
        asm volatile("global_load_dwordx4 %0, %1, %2" : "=&v"(rp[i]) : "v"(dv), "s"(rsrc[i]) : "memory");
    }
    grab(0);
    WAIT_VM_BARRIER(19); issue(4); grab(1);
    WAIT_VM_BARRIER(19); issue(5); grab(2);
    WAIT_VM_BARRIER(19); issue(6); grab(3);
    WAIT_VM_BARRIER(16); issue(7);
    {
      const size_t o0 = (size_t)gw * 1024, o1 = o0 + 768 * 1024;
      dma16(lds0 + K1_DUMMY, (const char*)wg2 + o0, lane * 16);
      dma16(lds0 + K1_DUMMY, (const char*)wu2 + o0, lane * 16);
      dma16(lds0 + K1_DUMMY, (const char*)wg2 + o1, lane * 16);
      dma16(lds0 + K1_DUMMY, (const char*)wu2 + o1, lane * 16);
    }
    grab(4);
    WAIT_VM_BARRIER(20); grab(5);
    WAIT_VM_BARRIER(12); grab(6);
    WAIT_VM_BARRIER(4); grab(7);
    asm volatile("" : "+v"(rp[0]), "+v"(rp[1]), "+v"(rp[2]));
    store_sc1(rdst[0] + lane * 16, rp[0]);
    store_sc1(rdst[1] + lane * 16, rp[1]);
    if (gw >= 768) store_sc1(rdst[2] + lane * 16, rp[2]);
    asm volatile("s_barrier" ::: "memory");
    asm volatile("s_barrier" ::: "memory");
    const int t = tid - 256;
    if (t < 64) {
      const int m_ = t >> 5, tk = t & 31;
      const float a = part[m_ * 192 + 64 + tk] + part[(m_ + 2) * 192 + 64 + tk];
      const float q = (part[m_ * 192 + tk] + part[m_ * 192 + 32 + tk]) + (part[(m_ + 2) * 192 + tk] + part[(m_ + 2) * 192 + 32 + tk]);
      const float mean = a * (1.f / 512.f);
      const float var = fmaxf(q * (1.f / 512.f) - mean * mean, 0.f);
      const float sd = bf_round(__builtin_amdgcn_sqrtf(bf_round(bf_round(var) + 1e-5f)));
      fin[t] = bf_round(mean);
      fin[64 + t] = __builtin_amdgcn_rcpf(sd);
    } else {
      const int i = t - 64, bb = i >> 6, p = (i >> 5) & 1, n = i & 31;
      fin[128 + i] = part[(2 * p) * 192 + 96 + bb * 32 + n] + part[(2 * p + 1) * 192 + 96 + bb * 32 + n];
    }
    asm volatile("s_waitcnt lgkmcnt(0)\n\ts_barrier" ::: "memory");
    const float mean = fin[xrow], rstd = fin[64 + xrow];
#pragma unroll
    for (int i = 0; i < 2; ++i) {
      u32x4 o;
#pragma unroll
      for (int e = 0; e < 4; ++e)
        o[e] = cvtpk(bf_round(bf_lo(keep[i][e]) - mean) * rstd, bf_round(bf_hi(keep[i][e]) - mean) * rstd);
      store_sc1(wsp + WS_XN + (size_t)(mblock * 32 + nj * 4 + hp * 2 + i) * 1024 + lane * 16, o);
    }
    asm volatile("s_waitcnt vmcnt(0)" ::: "memory");
    return;
  }

  {
    const int gw = b * 4 + w;
    dma16(lds0 + K1_DUMMY, (const char*)wqkv + (size_t)(gw + 384) * 1024, lane * 16);
    dma16(lds0 + K1_DUMMY, (const char*)wqkv + (size_t)(gw < 128 ? gw + 1408 : gw + 384) * 1024, lane * 16);
  }
  K1Addr a;
#pragma unroll
  for (int s = 0; s < 2; ++s) {
#pragma unroll
    for (int j = 0; j < 4; ++j) a.x[s][j] = lds0 + s * 65536u + xrow * 128 + (((2 * j + h) ^ xsw) << 4);
    const unsigned tr_r = (lane & 15) >> 2, tr_c = lane & 3;
    a.w[s] = lds0 + s * 65536u + (8 * h + tr_r) * 128 + ((hp ^ ((tr_r >> 1) & 1)) << 6) + ((lane >> 4) & 1) * 32 + tr_c * 8;
  }
  f32x16 acc[3];
#pragma unroll
  for (int i = 0; i < 3; ++i)
#pragma unroll
    for (int r = 0; r < 16; ++r) acc[i][r] = 0.f;
  asm volatile("s_nop 1" : "+v"(acc[0]), "+v"(acc[1]), "+v"(acc[2]));
  K1Sums S;
#pragma unroll
  for (int r = 0; r < 4; ++r) { S.cs[0][r] = 0.f; S.cs[1][r] = 0.f; S.cs[2][r] = 0.f; S.sx[r] = 0.f; }
  S.q0 = 0.f; S.q1 = 0.f;
  const unsigned selv = (lane == 0 || lane == 32 || lane == 17 || lane == 49) ? 0x3f803f80u : 0u;
  u32x4 sel = {selv, selv, selv, selv};
  asm volatile("s_nop 1" : "+v"(S.cs[0]), "+v"(S.cs[1]), "+v"(S.cs[2]), "+v"(S.sx), "+v"(sel));
  if (w == 0) k1_main<0, 0>(a, acc, S, sel);
  else if (w == 1) k1_main<1, 0>(a, acc, S, sel);
  else if (w == 2) k1_main<0, 1>(a, acc, S, sel);
  else k1_main<1, 1>(a, acc, S, sel);

  asm volatile("s_nop 7" : "+v"(S.cs[0]), "+v"(S.cs[1]), "+v"(S.cs[2]), "+v"(S.sx));
  part[w * 192 + lane] = S.q0 + S.q1;
  if (lane < 16) {
    part[w * 192 + 64 + lane] = S.sx[0];
    part[w * 192 + 80 + lane] = S.sx[1];
#pragma unroll
    for (int bb = 0; bb < 3; ++bb) {
      part[w * 192 + 96 + bb * 32 + lane] = S.cs[bb][0];
      part[w * 192 + 112 + bb * 32 + lane] = S.cs[bb][1];
    }
  }
  asm volatile("s_waitcnt vmcnt(0) lgkmcnt(0)\n\ts_barrier\n\ts_barrier" ::: "memory");
  asm volatile("s_nop 7" : "+v"(acc[0]), "+v"(acc[1]), "+v"(acc[2]));

  const float mean = fin[xrow], rstd = fin[64 + xrow];
#pragma unroll
  for (int bb = 0; bb < 2; ++bb) {
    const float sc = (bb == 0) ? rstd * 0.36067376022224085f : rstd;
    const float nms = -mean * sc;
    float v[16];
#pragma unroll
    for (int g = 0; g < 4; ++g) {
      const f32x4 c4 = *(LDS(f32x4)*)(fin + 128 + bb * 64 + hp * 32 + 8 * g + 4 * h);
#pragma unroll
      for (int i = 0; i < 4; ++i) v[4 * g + i] = acc[bb][4 * g + i] * sc + c4[i] * nms;
    }
#pragma unroll
    for (int hh = 0; hh < 2; ++hh) {
      const int head = nj * 4 + hp * 2 + hh;
      u32x4 o = {cvtpk(v[8 * hh + 0], v[8 * hh + 1]), cvtpk(v[8 * hh + 2], v[8 * hh + 3]),
                 cvtpk(v[8 * hh + 4], v[8 * hh + 5]), cvtpk(v[8 * hh + 6], v[8 * hh + 7])};
      char* dst = (bb == 0) ? wsp + WS_Q + (size_t)(head * 64 + mblock) * 1024 + lane * 16
                            : wsp + WS_KV + (size_t)(head * 64 + mblock) * 2048 + lane * 16;
      store_sc1(dst, o);
    }
  }
  {
    const float nc = -fin[128 + 128 + hp * 32 + l31];
    float v[16];
#pragma unroll
    for (int g = 0; g < 4; ++g) {
      const f32x4 m4 = *(LDS(f32x4)*)(fin + mh * 32 + 8 * g + 4 * h);
      const f32x4 r4 = *(LDS(f32x4)*)(fin + 64 + mh * 32 + 8 * g + 4 * h);
#pragma unroll
      for (int i = 0; i < 4; ++i) v[4 * g + i] = (acc[2][4 * g + i] + m4[i] * nc) * r4[i];
    }
    const int head = nj * 4 + hp * 2 + (l31 >> 4);
    char* dst = wsp + WS_KV + (size_t)(head * 64 + mblock) * 2048 + 1024 + h * 512 + (l31 & 15) * 16;
#pragma unroll
    for (int s = 0; s < 2; ++s) {
      u32x4 o = {cvtpk(v[8 * s + 0], v[8 * s + 1]), cvtpk(v[8 * s + 2], v[8 * s + 3]),
                 cvtpk(v[8 * s + 4], v[8 * s + 5]), cvtpk(v[8 * s + 6], v[8 * s + 7])};
      store_sc1(dst + s * 256, o);
    }
  }

}

#define ATTN_LOOP_ASM \
  "s_mov_b64 s[40:41], %[kvg]\n" \
  "s_mov_b32 s42, %[m0a]\n" \
  "s_mov_b32 s43, %[m0b]\n" \
  "s_mov_b32 s44, 0\n" \
  "ds_read_b128 v[112:115], v136 offset:0\n" \
  "ds_read_b128 v[120:123], v138 offset:1024\n" \
  "ds_read_b128 v[124:127], v138 offset:1280\n" \
  "ds_read_b128 v[116:119], v136 offset:2048\n" \
  "v_mov_b32 v132, 0\n" \
  "v_mov_b32 v133, 0\n" \
  "v_mov_b32 v134, 0\n" \
  "v_mov_b32 v135, 0\n" \
  "v_mov_b32 v76, 0\n" \
  "v_mov_b32 v77, 0\n" \
  "v_mov_b32 v78, 0\n" \
  "v_mov_b32 v79, 0\n" \
  "v_mov_b32 v80, 0\n" \
  "v_mov_b32 v81, 0\n" \
  "v_mov_b32 v82, 0\n" \
  "v_mov_b32 v83, 0\n" \
  "v_mov_b32 v84, 0\n" \
  "v_mov_b32 v85, 0\n" \
  "v_mov_b32 v86, 0\n" \
  "v_mov_b32 v87, 0\n" \
  "v_mov_b32 v88, 0\n" \
  "v_mov_b32 v89, 0\n" \
  "v_mov_b32 v90, 0\n" \
  "v_mov_b32 v91, 0\n" \
  "v_mov_b32 v92, 0\n" \
  "v_mov_b32 v93, 0\n" \
  "v_mov_b32 v94, 0\n" \
  "v_mov_b32 v95, 0\n" \
  "v_mov_b32 v96, 0\n" \
  "v_mov_b32 v97, 0\n" \
  "v_mov_b32 v98, 0\n" \
  "v_mov_b32 v99, 0\n" \
  "v_mov_b32 v100, 0\n" \
  "v_mov_b32 v101, 0\n" \
  "v_mov_b32 v102, 0\n" \
  "v_mov_b32 v103, 0\n" \
  "v_mov_b32 v104, 0\n" \
  "v_mov_b32 v105, 0\n" \
  "v_mov_b32 v106, 0\n" \
  "v_mov_b32 v107, 0\n" \
  "v_mov_b32 v108, 0\n" \
  "v_mov_b32 v109, 0\n" \
  "v_mov_b32 v110, 0\n" \
  "v_mov_b32 v111, 0\n" \
  "s_waitcnt lgkmcnt(0)\n" \
  "v_mfma_f32_32x32x16_bf16 v[40:55], v[112:115], v[0:3], v[8:23]\n" \
  "s_nop 15\n" \
  "1:\n" \
  "v_exp_f32 v40, v40\n" \
  "v_exp_f32 v41, v41\n" \
  "v_mfma_f32_32x32x16_bf16 v[96:111], v[132:135], v[76:79], v[96:111]\n" \
  "s_add_u32 s40, s40, 16384\n" \
  "s_addc_u32 s41, s41, 0\n" \
  "v_exp_f32 v42, v42\n" \
  "v_exp_f32 v43, v43\n" \
  "v_cvt_pk_bf16_f32 v72, v40, v41\n" \
  "v_exp_f32 v44, v44\n" \
  "v_exp_f32 v45, v45\n" \
  "v_cvt_pk_bf16_f32 v73, v42, v43\n" \
  "v_exp_f32 v46, v46\n" \
  "v_mfma_f32_32x32x16_bf16 v[56:71], v[112:115], v[4:7], v[24:39]\n" \
  "s_waitcnt lgkmcnt(0)\n" \
  "v_exp_f32 v47, v47\n" \
  "v_cvt_pk_bf16_f32 v74, v44, v45\n" \
  "v_exp_f32 v48, v48\n" \
  "v_cvt_pk_bf16_f32 v75, v46, v47\n" \
  "v_exp_f32 v49, v49\n" \
  "v_exp_f32 v50, v50\n" \
  "v_mfma_f32_32x32x16_bf16 v[80:95], v[120:123], v[72:75], v[80:95]\n" \
  "v_exp_f32 v51, v51\n" \
  "v_cvt_pk_bf16_f32 v76, v48, v49\n" \
  "v_exp_f32 v52, v52\n" \
  "v_exp_f32 v53, v53\n" \
  "v_cvt_pk_bf16_f32 v77, v50, v51\n" \
  "v_exp_f32 v54, v54\n" \
  "v_exp_f32 v55, v55\n" \
  "v_cvt_pk_bf16_f32 v78, v52, v53\n" \
  "v_cvt_pk_bf16_f32 v79, v54, v55\n" \
  "v_exp_f32 v56, v56\n" \
  "v_exp_f32 v57, v57\n" \
  "v_mfma_f32_32x32x16_bf16 v[80:95], v[124:127], v[76:79], v[80:95]\n" \
  "ds_read_b128 v[128:131], v138 offset:3072\n" \
  "ds_read_b128 v[132:135], v138 offset:3328\n" \
  "ds_read_b128 v[112:115], v136 offset:4096\n" \
  "v_exp_f32 v58, v58\n" \
  "v_exp_f32 v59, v59\n" \
  "v_cvt_pk_bf16_f32 v72, v56, v57\n" \
  "v_exp_f32 v60, v60\n" \
  "v_exp_f32 v61, v61\n" \
  "v_cvt_pk_bf16_f32 v73, v58, v59\n" \
  "v_exp_f32 v62, v62\n" \
  "v_mfma_f32_32x32x16_bf16 v[40:55], v[116:119], v[0:3], v[8:23]\n" \
  "v_exp_f32 v63, v63\n" \
  "v_cvt_pk_bf16_f32 v74, v60, v61\n" \
  "v_exp_f32 v64, v64\n" \
  "v_cvt_pk_bf16_f32 v75, v62, v63\n" \
  "v_exp_f32 v65, v65\n" \
  "v_exp_f32 v66, v66\n" \
  "v_mfma_f32_32x32x16_bf16 v[96:111], v[120:123], v[72:75], v[96:111]\n" \
  "v_exp_f32 v67, v67\n" \
  "v_cvt_pk_bf16_f32 v76, v64, v65\n" \
  "v_exp_f32 v68, v68\n" \
  "v_exp_f32 v69, v69\n" \
  "v_cvt_pk_bf16_f32 v77, v66, v67\n" \
  "v_exp_f32 v70, v70\n" \
  "v_exp_f32 v71, v71\n" \
  "v_cvt_pk_bf16_f32 v78, v68, v69\n" \
  "v_cvt_pk_bf16_f32 v79, v70, v71\n" \
  "v_exp_f32 v40, v40\n" \
  "v_exp_f32 v41, v41\n" \
  "v_mfma_f32_32x32x16_bf16 v[96:111], v[124:127], v[76:79], v[96:111]\n" \
  "v_exp_f32 v42, v42\n" \
  "v_exp_f32 v43, v43\n" \
  "v_cvt_pk_bf16_f32 v72, v40, v41\n" \
  "v_exp_f32 v44, v44\n" \
  "v_exp_f32 v45, v45\n" \
  "v_cvt_pk_bf16_f32 v73, v42, v43\n" \
  "v_exp_f32 v46, v46\n" \
  "v_mfma_f32_32x32x16_bf16 v[56:71], v[116:119], v[4:7], v[24:39]\n" \
  "s_waitcnt lgkmcnt(0)\n" \
  "v_exp_f32 v47, v47\n" \
  "v_cvt_pk_bf16_f32 v74, v44, v45\n" \
  "v_exp_f32 v48, v48\n" \
  "v_cvt_pk_bf16_f32 v75, v46, v47\n" \
  "v_exp_f32 v49, v49\n" \
  "v_exp_f32 v50, v50\n" \
  "v_mfma_f32_32x32x16_bf16 v[80:95], v[128:131], v[72:75], v[80:95]\n" \
  "v_exp_f32 v51, v51\n" \
  "v_cvt_pk_bf16_f32 v76, v48, v49\n" \
  "v_exp_f32 v52, v52\n" \
  "v_exp_f32 v53, v53\n" \
  "v_cvt_pk_bf16_f32 v77, v50, v51\n" \
  "v_exp_f32 v54, v54\n" \
  "v_exp_f32 v55, v55\n" \
  "v_cvt_pk_bf16_f32 v78, v52, v53\n" \
  "v_cvt_pk_bf16_f32 v79, v54, v55\n" \
  "v_exp_f32 v56, v56\n" \
  "v_exp_f32 v57, v57\n" \
  "v_mfma_f32_32x32x16_bf16 v[80:95], v[132:135], v[76:79], v[80:95]\n" \
  "ds_read_b128 v[120:123], v138 offset:5120\n" \
  "ds_read_b128 v[124:127], v138 offset:5376\n" \
  "ds_read_b128 v[116:119], v136 offset:6144\n" \
  "v_exp_f32 v58, v58\n" \
  "v_exp_f32 v59, v59\n" \
  "v_cvt_pk_bf16_f32 v72, v56, v57\n" \
  "v_exp_f32 v60, v60\n" \
  "v_exp_f32 v61, v61\n" \
  "v_cvt_pk_bf16_f32 v73, v58, v59\n" \
  "v_exp_f32 v62, v62\n" \
  "v_mfma_f32_32x32x16_bf16 v[40:55], v[112:115], v[0:3], v[8:23]\n" \
  "v_exp_f32 v63, v63\n" \
  "v_cvt_pk_bf16_f32 v74, v60, v61\n" \
  "v_exp_f32 v64, v64\n" \
  "v_cvt_pk_bf16_f32 v75, v62, v63\n" \
  "v_exp_f32 v65, v65\n" \
  "v_exp_f32 v66, v66\n" \
  "v_mfma_f32_32x32x16_bf16 v[96:111], v[128:131], v[72:75], v[96:111]\n" \
  "v_exp_f32 v67, v67\n" \
  "v_cvt_pk_bf16_f32 v76, v64, v65\n" \
  "v_exp_f32 v68, v68\n" \
  "v_exp_f32 v69, v69\n" \
  "v_cvt_pk_bf16_f32 v77, v66, v67\n" \
  "v_exp_f32 v70, v70\n" \
  "v_exp_f32 v71, v71\n" \
  "v_cvt_pk_bf16_f32 v78, v68, v69\n" \
  "v_cvt_pk_bf16_f32 v79, v70, v71\n" \
  "v_exp_f32 v40, v40\n" \
  "v_exp_f32 v41, v41\n" \
  "v_mfma_f32_32x32x16_bf16 v[96:111], v[132:135], v[76:79], v[96:111]\n" \
  "v_exp_f32 v42, v42\n" \
  "v_exp_f32 v43, v43\n" \
  "v_cvt_pk_bf16_f32 v72, v40, v41\n" \
  "v_exp_f32 v44, v44\n" \
  "v_exp_f32 v45, v45\n" \
  "v_cvt_pk_bf16_f32 v73, v42, v43\n" \
  "v_exp_f32 v46, v46\n" \
  "v_mfma_f32_32x32x16_bf16 v[56:71], v[112:115], v[4:7], v[24:39]\n" \
  "s_waitcnt lgkmcnt(0)\n" \
  "v_exp_f32 v47, v47\n" \
  "v_cvt_pk_bf16_f32 v74, v44, v45\n" \
  "v_exp_f32 v48, v48\n" \
  "v_cvt_pk_bf16_f32 v75, v46, v47\n" \
  "v_exp_f32 v49, v49\n" \
  "v_exp_f32 v50, v50\n" \
  "v_mfma_f32_32x32x16_bf16 v[80:95], v[120:123], v[72:75], v[80:95]\n" \
  "v_exp_f32 v51, v51\n" \
  "v_cvt_pk_bf16_f32 v76, v48, v49\n" \
  "v_exp_f32 v52, v52\n" \
  "v_exp_f32 v53, v53\n" \
  "v_cvt_pk_bf16_f32 v77, v50, v51\n" \
  "v_exp_f32 v54, v54\n" \
  "v_exp_f32 v55, v55\n" \
  "v_cvt_pk_bf16_f32 v78, v52, v53\n" \
  "v_cvt_pk_bf16_f32 v79, v54, v55\n" \
  "v_exp_f32 v56, v56\n" \
  "v_exp_f32 v57, v57\n" \
  "v_mfma_f32_32x32x16_bf16 v[80:95], v[124:127], v[76:79], v[80:95]\n" \
  "ds_read_b128 v[128:131], v138 offset:7168\n" \
  "ds_read_b128 v[132:135], v138 offset:7424\n" \
  "ds_read_b128 v[112:115], v136 offset:8192\n" \
  "v_exp_f32 v58, v58\n" \
  "v_exp_f32 v59, v59\n" \
  "v_cvt_pk_bf16_f32 v72, v56, v57\n" \
  "v_exp_f32 v60, v60\n" \
  "v_exp_f32 v61, v61\n" \
  "v_cvt_pk_bf16_f32 v73, v58, v59\n" \
  "v_exp_f32 v62, v62\n" \
  "v_mfma_f32_32x32x16_bf16 v[40:55], v[116:119], v[0:3], v[8:23]\n" \
  "v_exp_f32 v63, v63\n" \
  "v_cvt_pk_bf16_f32 v74, v60, v61\n" \
  "v_exp_f32 v64, v64\n" \
  "v_cvt_pk_bf16_f32 v75, v62, v63\n" \
  "v_exp_f32 v65, v65\n" \
  "v_exp_f32 v66, v66\n" \
  "v_mfma_f32_32x32x16_bf16 v[96:111], v[120:123], v[72:75], v[96:111]\n" \
  "v_exp_f32 v67, v67\n" \
  "v_cvt_pk_bf16_f32 v76, v64, v65\n" \
  "v_exp_f32 v68, v68\n" \
  "v_exp_f32 v69, v69\n" \
  "v_cvt_pk_bf16_f32 v77, v66, v67\n" \
  "v_exp_f32 v70, v70\n" \
  "v_exp_f32 v71, v71\n" \
  "v_cvt_pk_bf16_f32 v78, v68, v69\n" \
  "v_cvt_pk_bf16_f32 v79, v70, v71\n" \
  "v_exp_f32 v40, v40\n" \
  "v_exp_f32 v41, v41\n" \
  "v_mfma_f32_32x32x16_bf16 v[96:111], v[124:127], v[76:79], v[96:111]\n" \
  "v_exp_f32 v42, v42\n" \
  "v_exp_f32 v43, v43\n" \
  "v_cvt_pk_bf16_f32 v72, v40, v41\n" \
  "v_exp_f32 v44, v44\n" \
  "v_exp_f32 v45, v45\n" \
  "v_cvt_pk_bf16_f32 v73, v42, v43\n" \
  "v_exp_f32 v46, v46\n" \
  "v_mfma_f32_32x32x16_bf16 v[56:71], v[116:119], v[4:7], v[24:39]\n" \
  "s_waitcnt lgkmcnt(0)\n" \
  "v_exp_f32 v47, v47\n" \
  "v_cvt_pk_bf16_f32 v74, v44, v45\n" \
  "v_exp_f32 v48, v48\n" \
  "v_cvt_pk_bf16_f32 v75, v46, v47\n" \
  "v_exp_f32 v49, v49\n" \
  "v_exp_f32 v50, v50\n" \
  "v_mfma_f32_32x32x16_bf16 v[80:95], v[128:131], v[72:75], v[80:95]\n" \
  "v_exp_f32 v51, v51\n" \
  "v_cvt_pk_bf16_f32 v76, v48, v49\n" \
  "v_exp_f32 v52, v52\n" \
  "v_exp_f32 v53, v53\n" \
  "v_cvt_pk_bf16_f32 v77, v50, v51\n" \
  "v_exp_f32 v54, v54\n" \
  "v_exp_f32 v55, v55\n" \
  "v_cvt_pk_bf16_f32 v78, v52, v53\n" \
  "v_cvt_pk_bf16_f32 v79, v54, v55\n" \
  "v_exp_f32 v56, v56\n" \
  "v_exp_f32 v57, v57\n" \
  "v_mfma_f32_32x32x16_bf16 v[80:95], v[132:135], v[76:79], v[80:95]\n" \
  "ds_read_b128 v[120:123], v138 offset:9216\n" \
  "ds_read_b128 v[124:127], v138 offset:9472\n" \
  "ds_read_b128 v[116:119], v136 offset:10240\n" \
  "v_exp_f32 v58, v58\n" \
  "v_exp_f32 v59, v59\n" \
  "v_cvt_pk_bf16_f32 v72, v56, v57\n" \
  "v_exp_f32 v60, v60\n" \
  "v_exp_f32 v61, v61\n" \
  "v_cvt_pk_bf16_f32 v73, v58, v59\n" \
  "v_exp_f32 v62, v62\n" \
  "v_mfma_f32_32x32x16_bf16 v[40:55], v[112:115], v[0:3], v[8:23]\n" \
  "v_exp_f32 v63, v63\n" \
  "v_cvt_pk_bf16_f32 v74, v60, v61\n" \
  "v_exp_f32 v64, v64\n" \
  "v_cvt_pk_bf16_f32 v75, v62, v63\n" \
  "v_exp_f32 v65, v65\n" \
  "v_exp_f32 v66, v66\n" \
  "v_mfma_f32_32x32x16_bf16 v[96:111], v[128:131], v[72:75], v[96:111]\n" \
  "v_exp_f32 v67, v67\n" \
  "v_cvt_pk_bf16_f32 v76, v64, v65\n" \
  "v_exp_f32 v68, v68\n" \
  "v_exp_f32 v69, v69\n" \
  "v_cvt_pk_bf16_f32 v77, v66, v67\n" \
  "v_exp_f32 v70, v70\n" \
  "v_exp_f32 v71, v71\n" \
  "v_cvt_pk_bf16_f32 v78, v68, v69\n" \
  "v_cvt_pk_bf16_f32 v79, v70, v71\n" \
  "v_exp_f32 v40, v40\n" \
  "v_exp_f32 v41, v41\n" \
  "v_mfma_f32_32x32x16_bf16 v[96:111], v[132:135], v[76:79], v[96:111]\n" \
  "s_waitcnt vmcnt(4)\n" \
  "s_barrier\n" \
  "s_mov_b32 m0, s42\n" \
  "v_exp_f32 v42, v42\n" \
  "v_exp_f32 v43, v43\n" \
  "v_cvt_pk_bf16_f32 v72, v40, v41\n" \
  "v_exp_f32 v44, v44\n" \
  "v_exp_f32 v45, v45\n" \
  "v_cvt_pk_bf16_f32 v73, v42, v43\n" \
  "v_exp_f32 v46, v46\n" \
  "v_mfma_f32_32x32x16_bf16 v[56:71], v[112:115], v[4:7], v[24:39]\n" \
  "s_waitcnt lgkmcnt(0)\n" \
  "v_exp_f32 v47, v47\n" \
  "v_cvt_pk_bf16_f32 v74, v44, v45\n" \
  "v_exp_f32 v48, v48\n" \
  "v_cvt_pk_bf16_f32 v75, v46, v47\n" \
  "v_exp_f32 v49, v49\n" \
  "v_exp_f32 v50, v50\n" \
  "v_mfma_f32_32x32x16_bf16 v[80:95], v[120:123], v[72:75], v[80:95]\n" \
  "global_load_lds_dwordx4 v137, s[40:41] offset:0\n" \
  "v_exp_f32 v51, v51\n" \
  "v_cvt_pk_bf16_f32 v76, v48, v49\n" \
  "v_exp_f32 v52, v52\n" \
  "v_exp_f32 v53, v53\n" \
  "v_cvt_pk_bf16_f32 v77, v50, v51\n" \
  "v_exp_f32 v54, v54\n" \
  "v_exp_f32 v55, v55\n" \
  "v_cvt_pk_bf16_f32 v78, v52, v53\n" \
  "v_cvt_pk_bf16_f32 v79, v54, v55\n" \
  "v_exp_f32 v56, v56\n" \
  "v_exp_f32 v57, v57\n" \
  "v_mfma_f32_32x32x16_bf16 v[80:95], v[124:127], v[76:79], v[80:95]\n" \
  "ds_read_b128 v[128:131], v138 offset:11264\n" \
  "ds_read_b128 v[132:135], v138 offset:11520\n" \
  "ds_read_b128 v[112:115], v136 offset:12288\n" \
  "v_exp_f32 v58, v58\n" \
  "v_exp_f32 v59, v59\n" \
  "v_cvt_pk_bf16_f32 v72, v56, v57\n" \
  "v_exp_f32 v60, v60\n" \
  "v_exp_f32 v61, v61\n" \
  "v_cvt_pk_bf16_f32 v73, v58, v59\n" \
  "v_exp_f32 v62, v62\n" \
  "v_mfma_f32_32x32x16_bf16 v[40:55], v[116:119], v[0:3], v[8:23]\n" \
  "v_exp_f32 v63, v63\n" \
  "v_cvt_pk_bf16_f32 v74, v60, v61\n" \
  "v_exp_f32 v64, v64\n" \
  "v_cvt_pk_bf16_f32 v75, v62, v63\n" \
  "v_exp_f32 v65, v65\n" \
  "v_exp_f32 v66, v66\n" \
  "v_mfma_f32_32x32x16_bf16 v[96:111], v[120:123], v[72:75], v[96:111]\n" \
  "v_exp_f32 v67, v67\n" \
  "v_cvt_pk_bf16_f32 v76, v64, v65\n" \
  "v_exp_f32 v68, v68\n" \
  "v_exp_f32 v69, v69\n" \
  "v_cvt_pk_bf16_f32 v77, v66, v67\n" \
  "v_exp_f32 v70, v70\n" \
  "v_exp_f32 v71, v71\n" \
  "v_cvt_pk_bf16_f32 v78, v68, v69\n" \
  "v_cvt_pk_bf16_f32 v79, v70, v71\n" \
  "v_exp_f32 v40, v40\n" \
  "v_exp_f32 v41, v41\n" \
  "v_mfma_f32_32x32x16_bf16 v[96:111], v[124:127], v[76:79], v[96:111]\n" \
  "v_exp_f32 v42, v42\n" \
  "v_exp_f32 v43, v43\n" \
  "v_cvt_pk_bf16_f32 v72, v40, v41\n" \
  "v_exp_f32 v44, v44\n" \
  "v_exp_f32 v45, v45\n" \
  "v_cvt_pk_bf16_f32 v73, v42, v43\n" \
  "v_exp_f32 v46, v46\n" \
  "v_mfma_f32_32x32x16_bf16 v[56:71], v[116:119], v[4:7], v[24:39]\n" \
  "s_waitcnt lgkmcnt(0)\n" \
  "v_exp_f32 v47, v47\n" \
  "v_cvt_pk_bf16_f32 v74, v44, v45\n" \
  "v_exp_f32 v48, v48\n" \
  "v_cvt_pk_bf16_f32 v75, v46, v47\n" \
  "v_exp_f32 v49, v49\n" \
  "v_exp_f32 v50, v50\n" \
  "v_mfma_f32_32x32x16_bf16 v[80:95], v[128:131], v[72:75], v[80:95]\n" \
  "global_load_lds_dwordx4 v137, s[40:41] offset:1024\n" \
  "v_exp_f32 v51, v51\n" \
  "v_cvt_pk_bf16_f32 v76, v48, v49\n" \
  "v_exp_f32 v52, v52\n" \
  "v_exp_f32 v53, v53\n" \
  "v_cvt_pk_bf16_f32 v77, v50, v51\n" \
  "v_exp_f32 v54, v54\n" \
  "v_exp_f32 v55, v55\n" \
  "v_cvt_pk_bf16_f32 v78, v52, v53\n" \
  "v_cvt_pk_bf16_f32 v79, v54, v55\n" \
  "v_exp_f32 v56, v56\n" \
  "v_exp_f32 v57, v57\n" \
  "v_mfma_f32_32x32x16_bf16 v[80:95], v[132:135], v[76:79], v[80:95]\n" \
  "ds_read_b128 v[120:123], v138 offset:13312\n" \
  "ds_read_b128 v[124:127], v138 offset:13568\n" \
  "ds_read_b128 v[116:119], v136 offset:14336\n" \
  "v_exp_f32 v58, v58\n" \
  "v_exp_f32 v59, v59\n" \
  "v_cvt_pk_bf16_f32 v72, v56, v57\n" \
  "v_exp_f32 v60, v60\n" \
  "v_exp_f32 v61, v61\n" \
  "v_cvt_pk_bf16_f32 v73, v58, v59\n" \
  "v_exp_f32 v62, v62\n" \
  "v_mfma_f32_32x32x16_bf16 v[40:55], v[112:115], v[0:3], v[8:23]\n" \
  "v_exp_f32 v63, v63\n" \
  "v_cvt_pk_bf16_f32 v74, v60, v61\n" \
  "v_exp_f32 v64, v64\n" \
  "v_cvt_pk_bf16_f32 v75, v62, v63\n" \
  "v_exp_f32 v65, v65\n" \
  "v_exp_f32 v66, v66\n" \
  "v_mfma_f32_32x32x16_bf16 v[96:111], v[128:131], v[72:75], v[96:111]\n" \
  "v_exp_f32 v67, v67\n" \
  "v_cvt_pk_bf16_f32 v76, v64, v65\n" \
  "v_exp_f32 v68, v68\n" \
  "v_exp_f32 v69, v69\n" \
  "v_cvt_pk_bf16_f32 v77, v66, v67\n" \
  "v_exp_f32 v70, v70\n" \
  "v_exp_f32 v71, v71\n" \
  "v_cvt_pk_bf16_f32 v78, v68, v69\n" \
  "v_cvt_pk_bf16_f32 v79, v70, v71\n" \
  "v_exp_f32 v40, v40\n" \
  "v_exp_f32 v41, v41\n" \
  "v_mfma_f32_32x32x16_bf16 v[96:111], v[132:135], v[76:79], v[96:111]\n" \
  "v_exp_f32 v42, v42\n" \
  "v_exp_f32 v43, v43\n" \
  "v_cvt_pk_bf16_f32 v72, v40, v41\n" \
  "v_exp_f32 v44, v44\n" \
  "v_exp_f32 v45, v45\n" \
  "v_cvt_pk_bf16_f32 v73, v42, v43\n" \
  "v_exp_f32 v46, v46\n" \
  "v_mfma_f32_32x32x16_bf16 v[56:71], v[112:115], v[4:7], v[24:39]\n" \
  "s_waitcnt lgkmcnt(0)\n" \
  "v_exp_f32 v47, v47\n" \
  "v_cvt_pk_bf16_f32 v74, v44, v45\n" \
  "v_exp_f32 v48, v48\n" \
  "v_cvt_pk_bf16_f32 v75, v46, v47\n" \
  "v_exp_f32 v49, v49\n" \
  "v_exp_f32 v50, v50\n" \
  "v_mfma_f32_32x32x16_bf16 v[80:95], v[120:123], v[72:75], v[80:95]\n" \
  "global_load_lds_dwordx4 v137, s[40:41] offset:2048\n" \
  "v_exp_f32 v51, v51\n" \
  "v_cvt_pk_bf16_f32 v76, v48, v49\n" \
  "v_exp_f32 v52, v52\n" \
  "v_exp_f32 v53, v53\n" \
  "v_cvt_pk_bf16_f32 v77, v50, v51\n" \
  "v_exp_f32 v54, v54\n" \
  "v_exp_f32 v55, v55\n" \
  "v_cvt_pk_bf16_f32 v78, v52, v53\n" \
  "v_cvt_pk_bf16_f32 v79, v54, v55\n" \
  "v_exp_f32 v56, v56\n" \
  "v_exp_f32 v57, v57\n" \
  "v_mfma_f32_32x32x16_bf16 v[80:95], v[124:127], v[76:79], v[80:95]\n" \
  "ds_read_b128 v[128:131], v138 offset:15360\n" \
  "ds_read_b128 v[132:135], v138 offset:15616\n" \
  "ds_read_b128 v[112:115], v136 offset:16384\n" \
  "v_exp_f32 v58, v58\n" \
  "v_exp_f32 v59, v59\n" \
  "v_cvt_pk_bf16_f32 v72, v56, v57\n" \
  "v_exp_f32 v60, v60\n" \
  "v_exp_f32 v61, v61\n" \
  "v_cvt_pk_bf16_f32 v73, v58, v59\n" \
  "v_exp_f32 v62, v62\n" \
  "v_mfma_f32_32x32x16_bf16 v[40:55], v[116:119], v[0:3], v[8:23]\n" \
  "v_exp_f32 v63, v63\n" \
  "v_cvt_pk_bf16_f32 v74, v60, v61\n" \
  "v_exp_f32 v64, v64\n" \
  "v_cvt_pk_bf16_f32 v75, v62, v63\n" \
  "v_exp_f32 v65, v65\n" \
  "v_exp_f32 v66, v66\n" \
  "v_mfma_f32_32x32x16_bf16 v[96:111], v[120:123], v[72:75], v[96:111]\n" \
  "v_exp_f32 v67, v67\n" \
  "v_cvt_pk_bf16_f32 v76, v64, v65\n" \
  "v_exp_f32 v68, v68\n" \
  "v_exp_f32 v69, v69\n" \
  "v_cvt_pk_bf16_f32 v77, v66, v67\n" \
  "v_exp_f32 v70, v70\n" \
  "v_exp_f32 v71, v71\n" \
  "v_cvt_pk_bf16_f32 v78, v68, v69\n" \
  "v_cvt_pk_bf16_f32 v79, v70, v71\n" \
  "v_exp_f32 v40, v40\n" \
  "v_exp_f32 v41, v41\n" \
  "v_mfma_f32_32x32x16_bf16 v[96:111], v[124:127], v[76:79], v[96:111]\n" \
  "v_exp_f32 v42, v42\n" \
  "v_exp_f32 v43, v43\n" \
  "v_cvt_pk_bf16_f32 v72, v40, v41\n" \
  "v_exp_f32 v44, v44\n" \
  "v_exp_f32 v45, v45\n" \
  "v_cvt_pk_bf16_f32 v73, v42, v43\n" \
  "v_exp_f32 v46, v46\n" \
  "v_mfma_f32_32x32x16_bf16 v[56:71], v[116:119], v[4:7], v[24:39]\n" \
  "s_waitcnt lgkmcnt(0)\n" \
  "v_exp_f32 v47, v47\n" \
  "v_cvt_pk_bf16_f32 v74, v44, v45\n" \
  "v_exp_f32 v48, v48\n" \
  "v_cvt_pk_bf16_f32 v75, v46, v47\n" \
  "v_exp_f32 v49, v49\n" \
  "v_exp_f32 v50, v50\n" \
  "v_mfma_f32_32x32x16_bf16 v[80:95], v[128:131], v[72:75], v[80:95]\n" \
  "global_load_lds_dwordx4 v137, s[40:41] offset:3072\n" \
  "v_exp_f32 v51, v51\n" \
  "v_cvt_pk_bf16_f32 v76, v48, v49\n" \
  "v_exp_f32 v52, v52\n" \
  "v_exp_f32 v53, v53\n" \
  "v_cvt_pk_bf16_f32 v77, v50, v51\n" \
  "v_exp_f32 v54, v54\n" \
  "v_exp_f32 v55, v55\n" \
  "v_cvt_pk_bf16_f32 v78, v52, v53\n" \
  "v_cvt_pk_bf16_f32 v79, v54, v55\n" \
  "v_exp_f32 v56, v56\n" \
  "v_exp_f32 v57, v57\n" \
  "v_mfma_f32_32x32x16_bf16 v[80:95], v[132:135], v[76:79], v[80:95]\n" \
  "ds_read_b128 v[120:123], v139 offset:1024\n" \
  "ds_read_b128 v[124:127], v139 offset:1280\n" \
  "ds_read_b128 v[116:119], v136 offset:18432\n" \
  "v_exp_f32 v58, v58\n" \
  "v_exp_f32 v59, v59\n" \
  "v_cvt_pk_bf16_f32 v72, v56, v57\n" \
  "v_exp_f32 v60, v60\n" \
  "v_exp_f32 v61, v61\n" \
  "v_cvt_pk_bf16_f32 v73, v58, v59\n" \
  "v_exp_f32 v62, v62\n" \
  "v_mfma_f32_32x32x16_bf16 v[40:55], v[112:115], v[0:3], v[8:23]\n" \
  "v_exp_f32 v63, v63\n" \
  "v_cvt_pk_bf16_f32 v74, v60, v61\n" \
  "v_exp_f32 v64, v64\n" \
  "v_cvt_pk_bf16_f32 v75, v62, v63\n" \
  "v_exp_f32 v65, v65\n" \
  "v_exp_f32 v66, v66\n" \
  "v_mfma_f32_32x32x16_bf16 v[96:111], v[128:131], v[72:75], v[96:111]\n" \
  "v_exp_f32 v67, v67\n" \
  "v_cvt_pk_bf16_f32 v76, v64, v65\n" \
  "v_exp_f32 v68, v68\n" \
  "v_exp_f32 v69, v69\n" \
  "v_cvt_pk_bf16_f32 v77, v66, v67\n" \
  "v_exp_f32 v70, v70\n" \
  "v_exp_f32 v71, v71\n" \
  "v_cvt_pk_bf16_f32 v78, v68, v69\n" \
  "v_cvt_pk_bf16_f32 v79, v70, v71\n" \
  "v_exp_f32 v40, v40\n" \
  "v_exp_f32 v41, v41\n" \
  "v_mfma_f32_32x32x16_bf16 v[96:111], v[132:135], v[76:79], v[96:111]\n" \
  "s_add_u32 s40, s40, 16384\n" \
  "s_addc_u32 s41, s41, 0\n" \
  "v_exp_f32 v42, v42\n" \
  "v_exp_f32 v43, v43\n" \
  "v_cvt_pk_bf16_f32 v72, v40, v41\n" \
  "v_exp_f32 v44, v44\n" \
  "v_exp_f32 v45, v45\n" \
  "v_cvt_pk_bf16_f32 v73, v42, v43\n" \
  "v_exp_f32 v46, v46\n" \
  "v_mfma_f32_32x32x16_bf16 v[56:71], v[112:115], v[4:7], v[24:39]\n" \
  "s_waitcnt lgkmcnt(0)\n" \
  "v_exp_f32 v47, v47\n" \
  "v_cvt_pk_bf16_f32 v74, v44, v45\n" \
  "v_exp_f32 v48, v48\n" \
  "v_cvt_pk_bf16_f32 v75, v46, v47\n" \
  "v_exp_f32 v49, v49\n" \
  "v_exp_f32 v50, v50\n" \
  "v_mfma_f32_32x32x16_bf16 v[80:95], v[120:123], v[72:75], v[80:95]\n" \
  "v_exp_f32 v51, v51\n" \
  "v_cvt_pk_bf16_f32 v76, v48, v49\n" \
  "v_exp_f32 v52, v52\n" \
  "v_exp_f32 v53, v53\n" \
  "v_cvt_pk_bf16_f32 v77, v50, v51\n" \
  "v_exp_f32 v54, v54\n" \
  "v_exp_f32 v55, v55\n" \
  "v_cvt_pk_bf16_f32 v78, v52, v53\n" \
  "v_cvt_pk_bf16_f32 v79, v54, v55\n" \
  "v_exp_f32 v56, v56\n" \
  "v_exp_f32 v57, v57\n" \
  "v_mfma_f32_32x32x16_bf16 v[80:95], v[124:127], v[76:79], v[80:95]\n" \
  "ds_read_b128 v[128:131], v139 offset:3072\n" \
  "ds_read_b128 v[132:135], v139 offset:3328\n" \
  "ds_read_b128 v[112:115], v136 offset:20480\n" \
  "v_exp_f32 v58, v58\n" \
  "v_exp_f32 v59, v59\n" \
  "v_cvt_pk_bf16_f32 v72, v56, v57\n" \
  "v_exp_f32 v60, v60\n" \
  "v_exp_f32 v61, v61\n" \
  "v_cvt_pk_bf16_f32 v73, v58, v59\n" \
  "v_exp_f32 v62, v62\n" \
  "v_mfma_f32_32x32x16_bf16 v[40:55], v[116:119], v[0:3], v[8:23]\n" \
  "v_exp_f32 v63, v63\n" \
  "v_cvt_pk_bf16_f32 v74, v60, v61\n" \
  "v_exp_f32 v64, v64\n" \
  "v_cvt_pk_bf16_f32 v75, v62, v63\n" \
  "v_exp_f32 v65, v65\n" \
  "v_exp_f32 v66, v66\n" \
  "v_mfma_f32_32x32x16_bf16 v[96:111], v[120:123], v[72:75], v[96:111]\n" \
  "v_exp_f32 v67, v67\n" \
  "v_cvt_pk_bf16_f32 v76, v64, v65\n" \
  "v_exp_f32 v68, v68\n" \
  "v_exp_f32 v69, v69\n" \
  "v_cvt_pk_bf16_f32 v77, v66, v67\n" \
  "v_exp_f32 v70, v70\n" \
  "v_exp_f32 v71, v71\n" \
  "v_cvt_pk_bf16_f32 v78, v68, v69\n" \
  "v_cvt_pk_bf16_f32 v79, v70, v71\n" \
  "v_exp_f32 v40, v40\n" \
  "v_exp_f32 v41, v41\n" \
  "v_mfma_f32_32x32x16_bf16 v[96:111], v[124:127], v[76:79], v[96:111]\n" \
  "v_exp_f32 v42, v42\n" \
  "v_exp_f32 v43, v43\n" \
  "v_cvt_pk_bf16_f32 v72, v40, v41\n" \
  "v_exp_f32 v44, v44\n" \
  "v_exp_f32 v45, v45\n" \
  "v_cvt_pk_bf16_f32 v73, v42, v43\n" \
  "v_exp_f32 v46, v46\n" \
  "v_mfma_f32_32x32x16_bf16 v[56:71], v[116:119], v[4:7], v[24:39]\n" \
  "s_waitcnt lgkmcnt(0)\n" \
  "v_exp_f32 v47, v47\n" \
  "v_cvt_pk_bf16_f32 v74, v44, v45\n" \
  "v_exp_f32 v48, v48\n" \
  "v_cvt_pk_bf16_f32 v75, v46, v47\n" \
  "v_exp_f32 v49, v49\n" \
  "v_exp_f32 v50, v50\n" \
  "v_mfma_f32_32x32x16_bf16 v[80:95], v[128:131], v[72:75], v[80:95]\n" \
  "v_exp_f32 v51, v51\n" \
  "v_cvt_pk_bf16_f32 v76, v48, v49\n" \
  "v_exp_f32 v52, v52\n" \
  "v_exp_f32 v53, v53\n" \
  "v_cvt_pk_bf16_f32 v77, v50, v51\n" \
  "v_exp_f32 v54, v54\n" \
  "v_exp_f32 v55, v55\n" \
  "v_cvt_pk_bf16_f32 v78, v52, v53\n" \
  "v_cvt_pk_bf16_f32 v79, v54, v55\n" \
  "v_exp_f32 v56, v56\n" \
  "v_exp_f32 v57, v57\n" \
  "v_mfma_f32_32x32x16_bf16 v[80:95], v[132:135], v[76:79], v[80:95]\n" \
  "ds_read_b128 v[120:123], v139 offset:5120\n" \
  "ds_read_b128 v[124:127], v139 offset:5376\n" \
  "ds_read_b128 v[116:119], v136 offset:22528\n" \
  "v_exp_f32 v58, v58\n" \
  "v_exp_f32 v59, v59\n" \
  "v_cvt_pk_bf16_f32 v72, v56, v57\n" \
  "v_exp_f32 v60, v60\n" \
  "v_exp_f32 v61, v61\n" \
  "v_cvt_pk_bf16_f32 v73, v58, v59\n" \
  "v_exp_f32 v62, v62\n" \
  "v_mfma_f32_32x32x16_bf16 v[40:55], v[112:115], v[0:3], v[8:23]\n" \
  "v_exp_f32 v63, v63\n" \
  "v_cvt_pk_bf16_f32 v74, v60, v61\n" \
  "v_exp_f32 v64, v64\n" \
  "v_cvt_pk_bf16_f32 v75, v62, v63\n" \
  "v_exp_f32 v65, v65\n" \
  "v_exp_f32 v66, v66\n" \
  "v_mfma_f32_32x32x16_bf16 v[96:111], v[128:131], v[72:75], v[96:111]\n" \
  "v_exp_f32 v67, v67\n" \
  "v_cvt_pk_bf16_f32 v76, v64, v65\n" \
  "v_exp_f32 v68, v68\n" \
  "v_exp_f32 v69, v69\n" \
  "v_cvt_pk_bf16_f32 v77, v66, v67\n" \
  "v_exp_f32 v70, v70\n" \
  "v_exp_f32 v71, v71\n" \
  "v_cvt_pk_bf16_f32 v78, v68, v69\n" \
  "v_cvt_pk_bf16_f32 v79, v70, v71\n" \
  "v_exp_f32 v40, v40\n" \
  "v_exp_f32 v41, v41\n" \
  "v_mfma_f32_32x32x16_bf16 v[96:111], v[132:135], v[76:79], v[96:111]\n" \
  "v_exp_f32 v42, v42\n" \
  "v_exp_f32 v43, v43\n" \
  "v_cvt_pk_bf16_f32 v72, v40, v41\n" \
  "v_exp_f32 v44, v44\n" \
  "v_exp_f32 v45, v45\n" \
  "v_cvt_pk_bf16_f32 v73, v42, v43\n" \
  "v_exp_f32 v46, v46\n" \
  "v_mfma_f32_32x32x16_bf16 v[56:71], v[112:115], v[4:7], v[24:39]\n" \
  "s_waitcnt lgkmcnt(0)\n" \
  "v_exp_f32 v47, v47\n" \
  "v_cvt_pk_bf16_f32 v74, v44, v45\n" \
  "v_exp_f32 v48, v48\n" \
  "v_cvt_pk_bf16_f32 v75, v46, v47\n" \
  "v_exp_f32 v49, v49\n" \
  "v_exp_f32 v50, v50\n" \
  "v_mfma_f32_32x32x16_bf16 v[80:95], v[120:123], v[72:75], v[80:95]\n" \
  "v_exp_f32 v51, v51\n" \
  "v_cvt_pk_bf16_f32 v76, v48, v49\n" \
  "v_exp_f32 v52, v52\n" \
  "v_exp_f32 v53, v53\n" \
  "v_cvt_pk_bf16_f32 v77, v50, v51\n" \
  "v_exp_f32 v54, v54\n" \
  "v_exp_f32 v55, v55\n" \
  "v_cvt_pk_bf16_f32 v78, v52, v53\n" \
  "v_cvt_pk_bf16_f32 v79, v54, v55\n" \
  "v_exp_f32 v56, v56\n" \
  "v_exp_f32 v57, v57\n" \
  "v_mfma_f32_32x32x16_bf16 v[80:95], v[124:127], v[76:79], v[80:95]\n" \
  "ds_read_b128 v[128:131], v139 offset:7168\n" \
  "ds_read_b128 v[132:135], v139 offset:7424\n" \
  "ds_read_b128 v[112:115], v136 offset:24576\n" \
  "v_exp_f32 v58, v58\n" \
  "v_exp_f32 v59, v59\n" \
  "v_cvt_pk_bf16_f32 v72, v56, v57\n" \
  "v_exp_f32 v60, v60\n" \
  "v_exp_f32 v61, v61\n" \
  "v_cvt_pk_bf16_f32 v73, v58, v59\n" \
  "v_exp_f32 v62, v62\n" \
  "v_mfma_f32_32x32x16_bf16 v[40:55], v[116:119], v[0:3], v[8:23]\n" \
  "v_exp_f32 v63, v63\n" \
  "v_cvt_pk_bf16_f32 v74, v60, v61\n" \
  "v_exp_f32 v64, v64\n" \
  "v_cvt_pk_bf16_f32 v75, v62, v63\n" \
  "v_exp_f32 v65, v65\n" \
  "v_exp_f32 v66, v66\n" \
  "v_mfma_f32_32x32x16_bf16 v[96:111], v[120:123], v[72:75], v[96:111]\n" \
  "v_exp_f32 v67, v67\n" \
  "v_cvt_pk_bf16_f32 v76, v64, v65\n" \
  "v_exp_f32 v68, v68\n" \
  "v_exp_f32 v69, v69\n" \
  "v_cvt_pk_bf16_f32 v77, v66, v67\n" \
  "v_exp_f32 v70, v70\n" \
  "v_exp_f32 v71, v71\n" \
  "v_cvt_pk_bf16_f32 v78, v68, v69\n" \
  "v_cvt_pk_bf16_f32 v79, v70, v71\n" \
  "v_exp_f32 v40, v40\n" \
  "v_exp_f32 v41, v41\n" \
  "v_mfma_f32_32x32x16_bf16 v[96:111], v[124:127], v[76:79], v[96:111]\n" \
  "v_exp_f32 v42, v42\n" \
  "v_exp_f32 v43, v43\n" \
  "v_cvt_pk_bf16_f32 v72, v40, v41\n" \
  "v_exp_f32 v44, v44\n" \
  "v_exp_f32 v45, v45\n" \
  "v_cvt_pk_bf16_f32 v73, v42, v43\n" \
  "v_exp_f32 v46, v46\n" \
  "v_mfma_f32_32x32x16_bf16 v[56:71], v[116:119], v[4:7], v[24:39]\n" \
  "s_waitcnt lgkmcnt(0)\n" \
  "v_exp_f32 v47, v47\n" \
  "v_cvt_pk_bf16_f32 v74, v44, v45\n" \
  "v_exp_f32 v48, v48\n" \
  "v_cvt_pk_bf16_f32 v75, v46, v47\n" \
  "v_exp_f32 v49, v49\n" \
  "v_exp_f32 v50, v50\n" \
  "v_mfma_f32_32x32x16_bf16 v[80:95], v[128:131], v[72:75], v[80:95]\n" \
  "v_exp_f32 v51, v51\n" \
  "v_cvt_pk_bf16_f32 v76, v48, v49\n" \
  "v_exp_f32 v52, v52\n" \
  "v_exp_f32 v53, v53\n" \
  "v_cvt_pk_bf16_f32 v77, v50, v51\n" \
  "v_exp_f32 v54, v54\n" \
  "v_exp_f32 v55, v55\n" \
  "v_cvt_pk_bf16_f32 v78, v52, v53\n" \
  "v_cvt_pk_bf16_f32 v79, v54, v55\n" \
  "v_exp_f32 v56, v56\n" \
  "v_exp_f32 v57, v57\n" \
  "v_mfma_f32_32x32x16_bf16 v[80:95], v[132:135], v[76:79], v[80:95]\n" \
  "ds_read_b128 v[120:123], v139 offset:9216\n" \
  "ds_read_b128 v[124:127], v139 offset:9472\n" \
  "ds_read_b128 v[116:119], v136 offset:26624\n" \
  "v_exp_f32 v58, v58\n" \
  "v_exp_f32 v59, v59\n" \
  "v_cvt_pk_bf16_f32 v72, v56, v57\n" \
  "v_exp_f32 v60, v60\n" \
  "v_exp_f32 v61, v61\n" \
  "v_cvt_pk_bf16_f32 v73, v58, v59\n" \
  "v_exp_f32 v62, v62\n" \
  "v_mfma_f32_32x32x16_bf16 v[40:55], v[112:115], v[0:3], v[8:23]\n" \
  "v_exp_f32 v63, v63\n" \
  "v_cvt_pk_bf16_f32 v74, v60, v61\n" \
  "v_exp_f32 v64, v64\n" \
  "v_cvt_pk_bf16_f32 v75, v62, v63\n" \
  "v_exp_f32 v65, v65\n" \
  "v_exp_f32 v66, v66\n" \
  "v_mfma_f32_32x32x16_bf16 v[96:111], v[128:131], v[72:75], v[96:111]\n" \
  "v_exp_f32 v67, v67\n" \
  "v_cvt_pk_bf16_f32 v76, v64, v65\n" \
  "v_exp_f32 v68, v68\n" \
  "v_exp_f32 v69, v69\n" \
  "v_cvt_pk_bf16_f32 v77, v66, v67\n" \
  "v_exp_f32 v70, v70\n" \
  "v_exp_f32 v71, v71\n" \
  "v_cvt_pk_bf16_f32 v78, v68, v69\n" \
  "v_cvt_pk_bf16_f32 v79, v70, v71\n" \
  "v_exp_f32 v40, v40\n" \
  "v_exp_f32 v41, v41\n" \
  "v_mfma_f32_32x32x16_bf16 v[96:111], v[132:135], v[76:79], v[96:111]\n" \
  "s_waitcnt vmcnt(4)\n" \
  "s_barrier\n" \
  "s_mov_b32 m0, s43\n" \
  "v_exp_f32 v42, v42\n" \
  "v_exp_f32 v43, v43\n" \
  "v_cvt_pk_bf16_f32 v72, v40, v41\n" \
  "v_exp_f32 v44, v44\n" \
  "v_exp_f32 v45, v45\n" \
  "v_cvt_pk_bf16_f32 v73, v42, v43\n" \
  "v_exp_f32 v46, v46\n" \
  "v_mfma_f32_32x32x16_bf16 v[56:71], v[112:115], v[4:7], v[24:39]\n" \
  "s_waitcnt lgkmcnt(0)\n" \
  "v_exp_f32 v47, v47\n" \
  "v_cvt_pk_bf16_f32 v74, v44, v45\n" \
  "v_exp_f32 v48, v48\n" \
  "v_cvt_pk_bf16_f32 v75, v46, v47\n" \
  "v_exp_f32 v49, v49\n" \
  "v_exp_f32 v50, v50\n" \
  "v_mfma_f32_32x32x16_bf16 v[80:95], v[120:123], v[72:75], v[80:95]\n" \
  "global_load_lds_dwordx4 v137, s[40:41] offset:0\n" \
  "v_exp_f32 v51, v51\n" \
  "v_cvt_pk_bf16_f32 v76, v48, v49\n" \
  "v_exp_f32 v52, v52\n" \
  "v_exp_f32 v53, v53\n" \
  "v_cvt_pk_bf16_f32 v77, v50, v51\n" \
  "v_exp_f32 v54, v54\n" \
  "v_exp_f32 v55, v55\n" \
  "v_cvt_pk_bf16_f32 v78, v52, v53\n" \
  "v_cvt_pk_bf16_f32 v79, v54, v55\n" \
  "v_exp_f32 v56, v56\n" \
  "v_exp_f32 v57, v57\n" \
  "v_mfma_f32_32x32x16_bf16 v[80:95], v[124:127], v[76:79], v[80:95]\n" \
  "ds_read_b128 v[128:131], v139 offset:11264\n" \
  "ds_read_b128 v[132:135], v139 offset:11520\n" \
  "ds_read_b128 v[112:115], v136 offset:28672\n" \
  "v_exp_f32 v58, v58\n" \
  "v_exp_f32 v59, v59\n" \
  "v_cvt_pk_bf16_f32 v72, v56, v57\n" \
  "v_exp_f32 v60, v60\n" \
  "v_exp_f32 v61, v61\n" \
  "v_cvt_pk_bf16_f32 v73, v58, v59\n" \
  "v_exp_f32 v62, v62\n" \
  "v_mfma_f32_32x32x16_bf16 v[40:55], v[116:119], v[0:3], v[8:23]\n" \
  "v_exp_f32 v63, v63\n" \
  "v_cvt_pk_bf16_f32 v74, v60, v61\n" \
  "v_exp_f32 v64, v64\n" \
  "v_cvt_pk_bf16_f32 v75, v62, v63\n" \
  "v_exp_f32 v65, v65\n" \
  "v_exp_f32 v66, v66\n" \
  "v_mfma_f32_32x32x16_bf16 v[96:111], v[120:123], v[72:75], v[96:111]\n" \
  "v_exp_f32 v67, v67\n" \
  "v_cvt_pk_bf16_f32 v76, v64, v65\n" \
  "v_exp_f32 v68, v68\n" \
  "v_exp_f32 v69, v69\n" \
  "v_cvt_pk_bf16_f32 v77, v66, v67\n" \
  "v_exp_f32 v70, v70\n" \
  "v_exp_f32 v71, v71\n" \
  "v_cvt_pk_bf16_f32 v78, v68, v69\n" \
  "v_cvt_pk_bf16_f32 v79, v70, v71\n" \
  "v_exp_f32 v40, v40\n" \
  "v_exp_f32 v41, v41\n" \
  "v_mfma_f32_32x32x16_bf16 v[96:111], v[124:127], v[76:79], v[96:111]\n" \
  "v_exp_f32 v42, v42\n" \
  "v_exp_f32 v43, v43\n" \
  "v_cvt_pk_bf16_f32 v72, v40, v41\n" \
  "v_exp_f32 v44, v44\n" \
  "v_exp_f32 v45, v45\n" \
  "v_cvt_pk_bf16_f32 v73, v42, v43\n" \
  "v_exp_f32 v46, v46\n" \
  "v_mfma_f32_32x32x16_bf16 v[56:71], v[116:119], v[4:7], v[24:39]\n" \
  "s_waitcnt lgkmcnt(0)\n" \
  "v_exp_f32 v47, v47\n" \
  "v_cvt_pk_bf16_f32 v74, v44, v45\n" \
  "v_exp_f32 v48, v48\n" \
  "v_cvt_pk_bf16_f32 v75, v46, v47\n" \
  "v_exp_f32 v49, v49\n" \
  "v_exp_f32 v50, v50\n" \
  "v_mfma_f32_32x32x16_bf16 v[80:95], v[128:131], v[72:75], v[80:95]\n" \
  "global_load_lds_dwordx4 v137, s[40:41] offset:1024\n" \
  "v_exp_f32 v51, v51\n" \
  "v_cvt_pk_bf16_f32 v76, v48, v49\n" \
  "v_exp_f32 v52, v52\n" \
  "v_exp_f32 v53, v53\n" \
  "v_cvt_pk_bf16_f32 v77, v50, v51\n" \
  "v_exp_f32 v54, v54\n" \
  "v_exp_f32 v55, v55\n" \
  "v_cvt_pk_bf16_f32 v78, v52, v53\n" \
  "v_cvt_pk_bf16_f32 v79, v54, v55\n" \
  "v_exp_f32 v56, v56\n" \
  "v_exp_f32 v57, v57\n" \
  "v_mfma_f32_32x32x16_bf16 v[80:95], v[132:135], v[76:79], v[80:95]\n" \
  "ds_read_b128 v[120:123], v139 offset:13312\n" \
  "ds_read_b128 v[124:127], v139 offset:13568\n" \
  "ds_read_b128 v[116:119], v136 offset:30720\n" \
  "v_exp_f32 v58, v58\n" \
  "v_exp_f32 v59, v59\n" \
  "v_cvt_pk_bf16_f32 v72, v56, v57\n" \
  "v_exp_f32 v60, v60\n" \
  "v_exp_f32 v61, v61\n" \
  "v_cvt_pk_bf16_f32 v73, v58, v59\n" \
  "v_exp_f32 v62, v62\n" \
  "v_mfma_f32_32x32x16_bf16 v[40:55], v[112:115], v[0:3], v[8:23]\n" \
  "v_exp_f32 v63, v63\n" \
  "v_cvt_pk_bf16_f32 v74, v60, v61\n" \
  "v_exp_f32 v64, v64\n" \
  "v_cvt_pk_bf16_f32 v75, v62, v63\n" \
  "v_exp_f32 v65, v65\n" \
  "v_exp_f32 v66, v66\n" \
  "v_mfma_f32_32x32x16_bf16 v[96:111], v[128:131], v[72:75], v[96:111]\n" \
  "v_exp_f32 v67, v67\n" \
  "v_cvt_pk_bf16_f32 v76, v64, v65\n" \
  "v_exp_f32 v68, v68\n" \
  "v_exp_f32 v69, v69\n" \
  "v_cvt_pk_bf16_f32 v77, v66, v67\n" \
  "v_exp_f32 v70, v70\n" \
  "v_exp_f32 v71, v71\n" \
  "v_cvt_pk_bf16_f32 v78, v68, v69\n" \
  "v_cvt_pk_bf16_f32 v79, v70, v71\n" \
  "v_exp_f32 v40, v40\n" \
  "v_exp_f32 v41, v41\n" \
  "v_mfma_f32_32x32x16_bf16 v[96:111], v[132:135], v[76:79], v[96:111]\n" \
  "v_exp_f32 v42, v42\n" \
  "v_exp_f32 v43, v43\n" \
  "v_cvt_pk_bf16_f32 v72, v40, v41\n" \
  "v_exp_f32 v44, v44\n" \
  "v_exp_f32 v45, v45\n" \
  "v_cvt_pk_bf16_f32 v73, v42, v43\n" \
  "v_exp_f32 v46, v46\n" \
  "v_mfma_f32_32x32x16_bf16 v[56:71], v[112:115], v[4:7], v[24:39]\n" \
  "s_waitcnt lgkmcnt(0)\n" \
  "v_exp_f32 v47, v47\n" \
  "v_cvt_pk_bf16_f32 v74, v44, v45\n" \
  "v_exp_f32 v48, v48\n" \
  "v_cvt_pk_bf16_f32 v75, v46, v47\n" \
  "v_exp_f32 v49, v49\n" \
  "v_exp_f32 v50, v50\n" \
  "v_mfma_f32_32x32x16_bf16 v[80:95], v[120:123], v[72:75], v[80:95]\n" \
  "global_load_lds_dwordx4 v137, s[40:41] offset:2048\n" \
  "v_exp_f32 v51, v51\n" \
  "v_cvt_pk_bf16_f32 v76, v48, v49\n" \
  "v_exp_f32 v52, v52\n" \
  "v_exp_f32 v53, v53\n" \
  "v_cvt_pk_bf16_f32 v77, v50, v51\n" \
  "v_exp_f32 v54, v54\n" \
  "v_exp_f32 v55, v55\n" \
  "v_cvt_pk_bf16_f32 v78, v52, v53\n" \
  "v_cvt_pk_bf16_f32 v79, v54, v55\n" \
  "v_exp_f32 v56, v56\n" \
  "v_exp_f32 v57, v57\n" \
  "v_mfma_f32_32x32x16_bf16 v[80:95], v[124:127], v[76:79], v[80:95]\n" \
  "ds_read_b128 v[128:131], v139 offset:15360\n" \
  "ds_read_b128 v[132:135], v139 offset:15616\n" \
  "ds_read_b128 v[112:115], v141 offset:0\n" \
  "v_exp_f32 v58, v58\n" \
  "v_exp_f32 v59, v59\n" \
  "v_cvt_pk_bf16_f32 v72, v56, v57\n" \
  "v_exp_f32 v60, v60\n" \
  "v_exp_f32 v61, v61\n" \
  "v_cvt_pk_bf16_f32 v73, v58, v59\n" \
  "v_exp_f32 v62, v62\n" \
  "v_mfma_f32_32x32x16_bf16 v[40:55], v[116:119], v[0:3], v[8:23]\n" \
  "v_exp_f32 v63, v63\n" \
  "v_cvt_pk_bf16_f32 v74, v60, v61\n" \
  "v_exp_f32 v64, v64\n" \
  "v_cvt_pk_bf16_f32 v75, v62, v63\n" \
  "v_exp_f32 v65, v65\n" \
  "v_exp_f32 v66, v66\n" \
  "v_mfma_f32_32x32x16_bf16 v[96:111], v[120:123], v[72:75], v[96:111]\n" \
  "v_exp_f32 v67, v67\n" \
  "v_cvt_pk_bf16_f32 v76, v64, v65\n" \
  "v_exp_f32 v68, v68\n" \
  "v_exp_f32 v69, v69\n" \
  "v_cvt_pk_bf16_f32 v77, v66, v67\n" \
  "v_exp_f32 v70, v70\n" \
  "v_exp_f32 v71, v71\n" \
  "v_cvt_pk_bf16_f32 v78, v68, v69\n" \
  "v_cvt_pk_bf16_f32 v79, v70, v71\n" \
  "v_exp_f32 v40, v40\n" \
  "v_exp_f32 v41, v41\n" \
  "v_mfma_f32_32x32x16_bf16 v[96:111], v[124:127], v[76:79], v[96:111]\n" \
  "s_xor_b32 s42, s42, 0x8000\n" \
  "s_xor_b32 s43, s43, 0x8000\n" \
  "s_add_u32 s44, s44, 1\n" \
  "s_cmp_lt_u32 s44, 4\n" \
  "v_exp_f32 v42, v42\n" \
  "v_exp_f32 v43, v43\n" \
  "v_cvt_pk_bf16_f32 v72, v40, v41\n" \
  "v_exp_f32 v44, v44\n" \
  "v_exp_f32 v45, v45\n" \
  "v_cvt_pk_bf16_f32 v73, v42, v43\n" \
  "v_exp_f32 v46, v46\n" \
  "v_mfma_f32_32x32x16_bf16 v[56:71], v[116:119], v[4:7], v[24:39]\n" \
  "s_waitcnt lgkmcnt(0)\n" \
  "v_exp_f32 v47, v47\n" \
  "v_cvt_pk_bf16_f32 v74, v44, v45\n" \
  "v_exp_f32 v48, v48\n" \
  "v_cvt_pk_bf16_f32 v75, v46, v47\n" \
  "v_exp_f32 v49, v49\n" \
  "v_exp_f32 v50, v50\n" \
  "v_mfma_f32_32x32x16_bf16 v[80:95], v[128:131], v[72:75], v[80:95]\n" \
  "global_load_lds_dwordx4 v137, s[40:41] offset:3072\n" \
  "v_exp_f32 v51, v51\n" \
  "v_cvt_pk_bf16_f32 v76, v48, v49\n" \
  "v_exp_f32 v52, v52\n" \
  "v_exp_f32 v53, v53\n" \
  "v_cvt_pk_bf16_f32 v77, v50, v51\n" \
  "v_exp_f32 v54, v54\n" \
  "v_exp_f32 v55, v55\n" \
  "v_cvt_pk_bf16_f32 v78, v52, v53\n" \
  "v_cvt_pk_bf16_f32 v79, v54, v55\n" \
  "v_exp_f32 v56, v56\n" \
  "v_exp_f32 v57, v57\n" \
  "v_mfma_f32_32x32x16_bf16 v[80:95], v[132:135], v[76:79], v[80:95]\n" \
  "ds_read_b128 v[120:123], v140 offset:1024\n" \
  "ds_read_b128 v[124:127], v140 offset:1280\n" \
  "ds_read_b128 v[116:119], v141 offset:2048\n" \
  "v_exp_f32 v58, v58\n" \
  "v_exp_f32 v59, v59\n" \
  "v_cvt_pk_bf16_f32 v72, v56, v57\n" \
  "v_exp_f32 v60, v60\n" \
  "v_exp_f32 v61, v61\n" \
  "v_cvt_pk_bf16_f32 v73, v58, v59\n" \
  "v_exp_f32 v62, v62\n" \
  "v_mfma_f32_32x32x16_bf16 v[40:55], v[112:115], v[0:3], v[8:23]\n" \
  "v_exp_f32 v63, v63\n" \
  "v_xor_b32 v136, 0x8000, v136\n" \
  "v_xor_b32 v141, 0x8000, v141\n" \
  "v_cvt_pk_bf16_f32 v74, v60, v61\n" \
  "v_exp_f32 v64, v64\n" \
  "v_cvt_pk_bf16_f32 v75, v62, v63\n" \
  "v_exp_f32 v65, v65\n" \
  "v_exp_f32 v66, v66\n" \
  "v_mfma_f32_32x32x16_bf16 v[96:111], v[128:131], v[72:75], v[96:111]\n" \
  "v_exp_f32 v67, v67\n" \
  "v_xor_b32 v138, v142, v138\n" \
  "v_xor_b32 v139, v142, v139\n" \
  "v_xor_b32 v140, v142, v140\n" \
  "v_cvt_pk_bf16_f32 v76, v64, v65\n" \
  "v_exp_f32 v68, v68\n" \
  "v_exp_f32 v69, v69\n" \
  "v_cvt_pk_bf16_f32 v77, v66, v67\n" \
  "v_exp_f32 v70, v70\n" \
  "v_exp_f32 v71, v71\n" \
  "v_cvt_pk_bf16_f32 v78, v68, v69\n" \
  "v_cvt_pk_bf16_f32 v79, v70, v71\n" \
  "s_cbranch_scc1 1b\n" \
  "v_mfma_f32_32x32x16_bf16 v[96:111], v[132:135], v[76:79], v[96:111]\n" \
  "s_waitcnt vmcnt(0) lgkmcnt(0)\n" \
  "s_nop 15\n" \
  ""
constexpr float ATT_BIAS = 100.f;
constexpr int ATT_LDS = 65536;

DI void half_swap(float& a, float& b) { asm volatile("s_nop 1\n\tv_permlane32_swap_b32 %0, %1" : "+v"(a), "+v"(b)); }
DI float half_swap_max(float v) { float a = v, b = v; half_swap(a, b); return fmaxf(a, b); }
DI float low_half_bcast(float v) { float a = v, b = v; half_swap(a, b); return a; }
DI float max16(const f32x16& s) {
  float m = fmaxf(fmaxf(s[0], s[1]), s[2]);
#pragma unroll
  for (int i = 3; i < 15; i += 2) m = fmaxf(fmaxf(m, s[i]), s[i + 1]);
  return fmaxf(m, s[15]);
}

DI f32x16 attn_safe(const u32x4& q, const char* kv, int lane, unsigned vinit) {
  f32x16 o, z;
#pragma unroll
  for (int r = 0; r < 16; ++r) { o[r] = 0.f; z[r] = 0.f; }
  float m = -3.0e38f;
  const bool vl = (lane & 16) == 0;
#pragma unroll 1
  for (int j = 0; j < 64; ++j) {
    const char* blk = kv + j * 2048 + lane * 16;
    const u32x4 kf = *(const u32x4*)blk;
    u32x4 v0 = {vinit, vinit, vinit, vinit}, v1 = v0;
    if (vl) { v0 = *(const u32x4*)(blk + 1024); v1 = *(const u32x4*)(blk + 1280); }
    f32x16 s = mfma32(kf, q, z);
    const float mn = fmaxf(m, half_swap_max(max16(s)));
    const float al = __builtin_amdgcn_exp2f(m - mn);
    m = mn;
    u32x4 p0, p1;
#pragma unroll
    for (int r = 0; r < 16; ++r) { s[r] = __builtin_amdgcn_exp2f(s[r] - mn); o[r] *= al; }
#pragma unroll
    for (int i = 0; i < 4; ++i) { p0[i] = cvtpk(s[2 * i], s[2 * i + 1]); p1[i] = cvtpk(s[8 + 2 * i], s[9 + 2 * i]); }
    o = mfma32(v0, p0, o);
    o = mfma32(v1, p1, o);
  }
  return o;
}

DI bool attn_ok(const f32x16& o, float l) {
  const float mag = ((fabsf(o[0]) + fabsf(o[1])) + (fabsf(o[2]) + fabsf(o[3]))) + ((fabsf(o[4]) + fabsf(o[5])) + (fabsf(o[6]) + fabsf(o[7])));
  return mag < 3.0e38f && l >= 3.9443045e-31f && l <= 8.5070592e37f;
}

DI void attn_store(const f32x16& o, float l, char* dst) {
  const float rl = __builtin_amdgcn_rcpf(l);
  u32x4 r = {cvtpk(o[0] * rl, o[1] * rl), cvtpk(o[2] * rl, o[3] * rl), cvtpk(o[4] * rl, o[5] * rl), cvtpk(o[6] * rl, o[7] * rl)};
  store_sc1(dst, r);
}

DI void attn_role(int b, char* __restrict__ wsp, unsigned lds0) {
  const int tid = threadIdx.x, lane = tid & 63, w = rfl(tid >> 6);
  const int xcd = b & 7, idx = b >> 3;
  const int head = xcd + 8 * (idx >> 3), qt = idx & 7;
  const int mb = qt * 8 + 2 * w;
  const char* kv = wsp + WS_KV + (size_t)head * 64 * 2048;

  __builtin_amdgcn_s_setprio(1);
  unsigned dmaoff = w * 4096 + lane * 16;
  const unsigned m0b = lds0 + w * 4096;
  const char* qp = wsp + WS_Q + (size_t)(head * 64 + mb) * 1024 + lane * 16;
  u32x4 qa, qb, k0;
  asm volatile("global_load_dwordx4 %0, %3, off\n\tglobal_load_dwordx4 %1, %3, off offset:1024\n\tglobal_load_dwordx4 %2, %4, off"
               : "=&v"(qa), "=&v"(qb), "=&v"(k0) : "v"(qp), "v"(kv + lane * 16) : "memory");
#pragma unroll
  for (int sg = 0; sg < 3; ++sg)
#pragma unroll
    for (int k = 0; k < 4; ++k) dma16(m0b + sg * 16384 + k * 1024, kv + sg * 16384 + k * 1024, dmaoff);
  dmaoff += 2 * 16384;
  if (tid < 32) {
    const unsigned one = (tid & 1) ? 0x3f803f80u : 0u;
    *(LDS(u32x4)*)(lds0 + ATT_LDS + (tid >> 2) * 2048 + ((tid >> 1) & 1) * 256 + (tid & 1) * 16) = u32x4{one, one, one, one};
  }
  asm volatile("s_waitcnt vmcnt(12)" : "+v"(qa), "+v"(qb), "+v"(k0) :: "memory");

  const unsigned laneb = lds0 + lane * 16;
  const unsigned vinit = ((lane & 31) == 16) ? 0x3f803f80u : 0u;
  f32x16 nma, nmb;
  {
    f32x16 z;
#pragma unroll
    for (int r = 0; r < 16; ++r) z[r] = 0.f;
    const float ma = half_swap_max(max16(mfma32(k0, qa, z))), mb_ = half_swap_max(max16(mfma32(k0, qb, z)));
#pragma unroll
    for (int r = 0; r < 16; ++r) { nma[r] = -(ma + ATT_BIAS); nmb[r] = -(mb_ + ATT_BIAS); }
  }
  asm volatile("s_waitcnt vmcnt(8) lgkmcnt(0)\n\ts_barrier" : "+v"(nma), "+v"(nmb) :: "memory");
  f32x16 oa, ob;
  const bool vlane = (lane & 16) == 0;
  const unsigned vconst = lds0 + ATT_LDS - 1024 + ((lane & 31) == 16 ? 16u : 0u);
  unsigned kcur = laneb, knxt = laneb + 32768;
  unsigned va0 = vlane ? laneb : vconst, va1 = vlane ? laneb + 16384 : vconst, va2 = vlane ? laneb + 32768 : vconst;
  const unsigned vmask = vlane ? 0x8000u : 0u;

  asm volatile(ATTN_LOOP_ASM
               : "={v[80:95]}"(oa), "={v[96:111]}"(ob), "+{v136}"(kcur), "+{v141}"(knxt), "+{v138}"(va0), "+{v139}"(va1), "+{v140}"(va2)
               : "{v[0:3]}"(qa), "{v[4:7]}"(qb), "{v[8:23]}"(nma), "{v[24:39]}"(nmb), [m0a] "s"(m0b + 49152), [m0b] "s"(m0b), [kvg] "s"(kv), "{v137}"(dmaoff), "{v142}"(vmask)
               : "memory", "scc", "s40", "s41", "s42", "s43", "s44", "s46", "s47",
                 "v40", "v41", "v42", "v43", "v44", "v45", "v46", "v47", "v48", "v49", "v50", "v51", "v52", "v53", "v54", "v55",
                 "v56", "v57", "v58", "v59", "v60", "v61", "v62", "v63", "v64", "v65", "v66", "v67", "v68", "v69", "v70", "v71",
                 "v72", "v73", "v74", "v75", "v76", "v77", "v78", "v79",
                 "v112", "v113", "v114", "v115", "v116", "v117", "v118", "v119", "v120", "v121", "v122", "v123",
                 "v124", "v125", "v126", "v127", "v128", "v129", "v130", "v131", "v132", "v133", "v134", "v135");

  float la = low_half_bcast(oa[8]), lb = low_half_bcast(ob[8]);
  const bool ok = attn_ok(oa, la) && attn_ok(ob, lb);
  if (__builtin_amdgcn_ballot_w64(!ok) != 0) {
    oa = attn_safe(qa, kv, lane, vinit);
    ob = attn_safe(qb, kv, lane, vinit);
    la = low_half_bcast(oa[8]);
    lb = low_half_bcast(ob[8]);
  }
  char* od = wsp + WS_O + (size_t)(mb * 32 + head) * 1024 + lane * 16;
  attn_store(oa, la, od);
  attn_store(ob, lb, od + 32 * 1024);
  asm volatile("s_waitcnt vmcnt(0)\n\ts_mov_b64 exec, 1\n\tglobal_atomic_add %0, %1, %2\n\ts_mov_b64 exec, -1"
               :: "v"(0u), "v"(1u), "s"(wsp + WS_CNT + (16 + qt * 4 + w) * 256) : "memory");

}

#define G1_ASM \
  "s_mov_b64 s[40:41], %[wb0]\n" \
  "s_mov_b64 s[42:43], %[xb0]\n" \
  "s_mov_b64 exec, %[dmask]\n" \
  "s_add_u32 m0, %[m0b], 0\n" \
  "s_nop 0\n" \
  "global_load_lds_dwordx4 v202, s[40:41]\n" \
  "s_mov_b64 exec, -1\n" \
  "s_mov_b64 exec, %[dmask]\n" \
  "s_add_u32 m0, %[m0b], 896\n" \
  "s_nop 0\n" \
  "global_load_lds_dwordx4 v203, s[40:41]\n" \
  "s_mov_b64 exec, -1\n" \
  "s_mov_b64 exec, %[dmask]\n" \
  "s_add_u32 m0, %[m0b], 1792\n" \
  "s_nop 0\n" \
  "global_load_lds_dwordx4 v204, s[40:41]\n" \
  "s_mov_b64 exec, -1\n" \
  "s_mov_b64 exec, %[dmask]\n" \
  "s_add_u32 m0, %[m0b], 2688\n" \
  "s_nop 0\n" \
  "global_load_lds_dwordx4 v205, s[40:41]\n" \
  "s_mov_b64 exec, -1\n" \
  "s_add_u32 s40, s40, 114688\n" \
  "s_addc_u32 s41, s41, 0\n" \
  "global_load_dwordx4 v[168:171], v201, s[42:43]\n" \
  "s_add_u32 s42, s42, 1024\n" \
  "s_addc_u32 s43, s43, 0\n" \
  "global_load_dwordx4 v[172:175], v201, s[42:43]\n" \
  "s_add_u32 s42, s42, 1024\n" \
  "s_addc_u32 s43, s43, 0\n" \
  "s_mov_b64 exec, %[dmask]\n" \
  "s_add_u32 m0, %[m0b], 14592\n" \
  "s_nop 0\n" \
  "global_load_lds_dwordx4 v202, s[40:41]\n" \
  "s_mov_b64 exec, -1\n" \
  "s_mov_b64 exec, %[dmask]\n" \
  "s_add_u32 m0, %[m0b], 15488\n" \
  "s_nop 0\n" \
  "global_load_lds_dwordx4 v203, s[40:41]\n" \
  "s_mov_b64 exec, -1\n" \
  "s_mov_b64 exec, %[dmask]\n" \
  "s_add_u32 m0, %[m0b], 16384\n" \
  "s_nop 0\n" \
  "global_load_lds_dwordx4 v204, s[40:41]\n" \
  "s_mov_b64 exec, -1\n" \
  "s_mov_b64 exec, %[dmask]\n" \
  "s_add_u32 m0, %[m0b], 17280\n" \
  "s_nop 0\n" \
  "global_load_lds_dwordx4 v205, s[40:41]\n" \
  "s_mov_b64 exec, -1\n" \
  "s_add_u32 s40, s40, 114688\n" \
  "s_addc_u32 s41, s41, 0\n" \
  "global_load_dwordx4 v[176:179], v201, s[42:43]\n" \
  "s_add_u32 s42, s42, 1024\n" \
  "s_addc_u32 s43, s43, 0\n" \
  "global_load_dwordx4 v[180:183], v201, s[42:43]\n" \
  "s_add_u32 s42, s42, 1024\n" \
  "s_addc_u32 s43, s43, 0\n" \
  "s_mov_b64 exec, %[dmask]\n" \
  "s_add_u32 m0, %[m0b], 29184\n" \
  "s_nop 0\n" \
  "global_load_lds_dwordx4 v202, s[40:41]\n" \
  "s_mov_b64 exec, -1\n" \
  "s_mov_b64 exec, %[dmask]\n" \
  "s_add_u32 m0, %[m0b], 30080\n" \
  "s_nop 0\n" \
  "global_load_lds_dwordx4 v203, s[40:41]\n" \
  "s_mov_b64 exec, -1\n" \
  "s_mov_b64 exec, %[dmask]\n" \
  "s_add_u32 m0, %[m0b], 30976\n" \
  "s_nop 0\n" \
  "global_load_lds_dwordx4 v204, s[40:41]\n" \
  "s_mov_b64 exec, -1\n" \
  "s_mov_b64 exec, %[dmask]\n" \
  "s_add_u32 m0, %[m0b], 31872\n" \
  "s_nop 0\n" \
  "global_load_lds_dwordx4 v205, s[40:41]\n" \
  "s_mov_b64 exec, -1\n" \
  "s_add_u32 s40, s40, 114688\n" \
  "s_addc_u32 s41, s41, 0\n" \
  "global_load_dwordx4 v[184:187], v201, s[42:43]\n" \
  "s_add_u32 s42, s42, 1024\n" \
  "s_addc_u32 s43, s43, 0\n" \
  "global_load_dwordx4 v[188:191], v201, s[42:43]\n" \
  "s_add_u32 s42, s42, 1024\n" \
  "s_addc_u32 s43, s43, 0\n" \
  "s_mov_b64 exec, %[dmask]\n" \
  "s_add_u32 m0, %[m0b], 43776\n" \
  "s_nop 0\n" \
  "global_load_lds_dwordx4 v202, s[40:41]\n" \
  "s_mov_b64 exec, -1\n" \
  "s_mov_b64 exec, %[dmask]\n" \
  "s_add_u32 m0, %[m0b], 44672\n" \
  "s_nop 0\n" \
  "global_load_lds_dwordx4 v203, s[40:41]\n" \
  "s_mov_b64 exec, -1\n" \
  "s_mov_b64 exec, %[dmask]\n" \
  "s_add_u32 m0, %[m0b], 45568\n" \
  "s_nop 0\n" \
  "global_load_lds_dwordx4 v204, s[40:41]\n" \
  "s_mov_b64 exec, -1\n" \
  "s_mov_b64 exec, %[dmask]\n" \
  "s_add_u32 m0, %[m0b], 46464\n" \
  "s_nop 0\n" \
  "global_load_lds_dwordx4 v205, s[40:41]\n" \
  "s_mov_b64 exec, -1\n" \
  "s_add_u32 s40, s40, 114688\n" \
  "s_addc_u32 s41, s41, 0\n" \
  "global_load_dwordx4 v[192:195], v201, s[42:43]\n" \
  "s_add_u32 s42, s42, 1024\n" \
  "s_addc_u32 s43, s43, 0\n" \
  "global_load_dwordx4 v[196:199], v201, s[42:43]\n" \
  "s_add_u32 s42, s42, 1024\n" \
  "s_addc_u32 s43, s43, 0\n" \
  "s_waitcnt vmcnt(18)\n" \
  "s_barrier\n" \
  "ds_read_b64_tr_b16 v[112:113], v200 offset:0\n" \
  "ds_read_b64_tr_b16 v[114:115], v200 offset:896\n" \
  "ds_read_b64_tr_b16 v[116:117], v200 offset:32\n" \
  "ds_read_b64_tr_b16 v[118:119], v200 offset:928\n" \
  "ds_read_b64_tr_b16 v[120:121], v200 offset:64\n" \
  "ds_read_b64_tr_b16 v[122:123], v200 offset:960\n" \
  "ds_read_b64_tr_b16 v[124:125], v200 offset:96\n" \
  "ds_read_b64_tr_b16 v[126:127], v200 offset:992\n" \
  "ds_read_b64_tr_b16 v[128:129], v200 offset:128\n" \
  "ds_read_b64_tr_b16 v[130:131], v200 offset:1024\n" \
  "ds_read_b64_tr_b16 v[132:133], v200 offset:160\n" \
  "ds_read_b64_tr_b16 v[134:135], v200 offset:1056\n" \
  "ds_read_b64_tr_b16 v[136:137], v200 offset:192\n" \
  "ds_read_b64_tr_b16 v[138:139], v200 offset:1088\n" \
  "s_waitcnt lgkmcnt(0)\n" \
  "v_mfma_f32_32x32x16_bf16 v[0:15], v[112:115], v[168:171], 0\n" \
  "ds_read_b64_tr_b16 v[140:141], v200 offset:3584\n" \
  "ds_read_b64_tr_b16 v[142:143], v200 offset:4480\n" \
  "ds_read_b64_tr_b16 v[144:145], v200 offset:3616\n" \
  "v_mfma_f32_32x32x16_bf16 v[16:31], v[116:119], v[168:171], 0\n" \
  "ds_read_b64_tr_b16 v[146:147], v200 offset:4512\n" \
  "ds_read_b64_tr_b16 v[148:149], v200 offset:3648\n" \
  "ds_read_b64_tr_b16 v[150:151], v200 offset:4544\n" \
  "v_mfma_f32_32x32x16_bf16 v[32:47], v[120:123], v[168:171], 0\n" \
  "ds_read_b64_tr_b16 v[152:153], v200 offset:3680\n" \
  "ds_read_b64_tr_b16 v[154:155], v200 offset:4576\n" \
  "ds_read_b64_tr_b16 v[156:157], v200 offset:3712\n" \
  "v_mfma_f32_32x32x16_bf16 v[48:63], v[124:127], v[168:171], 0\n" \
  "ds_read_b64_tr_b16 v[158:159], v200 offset:4608\n" \
  "ds_read_b64_tr_b16 v[160:161], v200 offset:3744\n" \
  "ds_read_b64_tr_b16 v[162:163], v200 offset:4640\n" \
  "v_mfma_f32_32x32x16_bf16 v[64:79], v[128:131], v[168:171], 0\n" \
  "ds_read_b64_tr_b16 v[164:165], v200 offset:3776\n" \
  "ds_read_b64_tr_b16 v[166:167], v200 offset:4672\n" \
  "v_mfma_f32_32x32x16_bf16 v[80:95], v[132:135], v[168:171], 0\n" \
  "v_mfma_f32_32x32x16_bf16 v[96:111], v[136:139], v[168:171], 0\n" \
  "global_load_dwordx4 v[168:171], v201, s[42:43]\n" \
  "s_add_u32 s42, s42, 1024\n" \
  "s_addc_u32 s43, s43, 0\n" \
  "s_waitcnt lgkmcnt(0)\n" \
  "s_waitcnt vmcnt(13)\n" \
  "s_barrier\n" \
  "v_mfma_f32_32x32x16_bf16 v[0:15], v[140:143], v[172:175], v[0:15]\n" \
  "ds_read_b64_tr_b16 v[112:113], v200 offset:14592\n" \
  "ds_read_b64_tr_b16 v[114:115], v200 offset:15488\n" \
  "ds_read_b64_tr_b16 v[116:117], v200 offset:14624\n" \
  "v_mfma_f32_32x32x16_bf16 v[16:31], v[144:147], v[172:175], v[16:31]\n" \
  "ds_read_b64_tr_b16 v[118:119], v200 offset:15520\n" \
  "ds_read_b64_tr_b16 v[120:121], v200 offset:14656\n" \
  "ds_read_b64_tr_b16 v[122:123], v200 offset:15552\n" \
  "v_mfma_f32_32x32x16_bf16 v[32:47], v[148:151], v[172:175], v[32:47]\n" \
  "ds_read_b64_tr_b16 v[124:125], v200 offset:14688\n" \
  "ds_read_b64_tr_b16 v[126:127], v200 offset:15584\n" \
  "ds_read_b64_tr_b16 v[128:129], v200 offset:14720\n" \
  "v_mfma_f32_32x32x16_bf16 v[48:63], v[152:155], v[172:175], v[48:63]\n" \
  "ds_read_b64_tr_b16 v[130:131], v200 offset:15616\n" \
  "ds_read_b64_tr_b16 v[132:133], v200 offset:14752\n" \
  "ds_read_b64_tr_b16 v[134:135], v200 offset:15648\n" \
  "s_mov_b64 exec, %[dmask]\n" \
  "s_add_u32 m0, %[m0b], 0\n" \
  "s_nop 0\n" \
  "global_load_lds_dwordx4 v202, s[40:41]\n" \
  "s_mov_b64 exec, -1\n" \
  "v_mfma_f32_32x32x16_bf16 v[64:79], v[156:159], v[172:175], v[64:79]\n" \
  "ds_read_b64_tr_b16 v[136:137], v200 offset:14784\n" \
  "ds_read_b64_tr_b16 v[138:139], v200 offset:15680\n" \
  "s_mov_b64 exec, %[dmask]\n" \
  "s_add_u32 m0, %[m0b], 896\n" \
  "s_nop 0\n" \
  "global_load_lds_dwordx4 v203, s[40:41]\n" \
  "s_mov_b64 exec, -1\n" \
  "v_mfma_f32_32x32x16_bf16 v[80:95], v[160:163], v[172:175], v[80:95]\n" \
  "s_mov_b64 exec, %[dmask]\n" \
  "s_add_u32 m0, %[m0b], 1792\n" \
  "s_nop 0\n" \
  "global_load_lds_dwordx4 v204, s[40:41]\n" \
  "s_mov_b64 exec, -1\n" \
  "v_mfma_f32_32x32x16_bf16 v[96:111], v[164:167], v[172:175], v[96:111]\n" \
  "s_mov_b64 exec, %[dmask]\n" \
  "s_add_u32 m0, %[m0b], 2688\n" \
  "s_nop 0\n" \
  "global_load_lds_dwordx4 v205, s[40:41]\n" \
  "s_mov_b64 exec, -1\n" \
  "s_add_u32 s40, s40, 114688\n" \
  "s_addc_u32 s41, s41, 0\n" \
  "global_load_dwordx4 v[172:175], v201, s[42:43]\n" \
  "s_add_u32 s42, s42, 1024\n" \
  "s_addc_u32 s43, s43, 0\n" \
  "s_waitcnt lgkmcnt(0)\n" \
  "v_mfma_f32_32x32x16_bf16 v[0:15], v[112:115], v[176:179], v[0:15]\n" \
  "ds_read_b64_tr_b16 v[140:141], v200 offset:18176\n" \
  "ds_read_b64_tr_b16 v[142:143], v200 offset:19072\n" \
  "ds_read_b64_tr_b16 v[144:145], v200 offset:18208\n" \
  "v_mfma_f32_32x32x16_bf16 v[16:31], v[116:119], v[176:179], v[16:31]\n" \
  "ds_read_b64_tr_b16 v[146:147], v200 offset:19104\n" \
  "ds_read_b64_tr_b16 v[148:149], v200 offset:18240\n" \
  "ds_read_b64_tr_b16 v[150:151], v200 offset:19136\n" \
  "v_mfma_f32_32x32x16_bf16 v[32:47], v[120:123], v[176:179], v[32:47]\n" \
  "ds_read_b64_tr_b16 v[152:153], v200 offset:18272\n" \
  "ds_read_b64_tr_b16 v[154:155], v200 offset:19168\n" \
  "ds_read_b64_tr_b16 v[156:157], v200 offset:18304\n" \
  "v_mfma_f32_32x32x16_bf16 v[48:63], v[124:127], v[176:179], v[48:63]\n" \
  "ds_read_b64_tr_b16 v[158:159], v200 offset:19200\n" \
  "ds_read_b64_tr_b16 v[160:161], v200 offset:18336\n" \
  "ds_read_b64_tr_b16 v[162:163], v200 offset:19232\n" \
  "v_mfma_f32_32x32x16_bf16 v[64:79], v[128:131], v[176:179], v[64:79]\n" \
  "ds_read_b64_tr_b16 v[164:165], v200 offset:18368\n" \
  "ds_read_b64_tr_b16 v[166:167], v200 offset:19264\n" \
  "v_mfma_f32_32x32x16_bf16 v[80:95], v[132:135], v[176:179], v[80:95]\n" \
  "v_mfma_f32_32x32x16_bf16 v[96:111], v[136:139], v[176:179], v[96:111]\n" \
  "global_load_dwordx4 v[176:179], v201, s[42:43]\n" \
  "s_add_u32 s42, s42, 1024\n" \
  "s_addc_u32 s43, s43, 0\n" \
  "s_waitcnt lgkmcnt(0)\n" \
  "s_waitcnt vmcnt(13)\n" \
  "s_barrier\n" \
  "v_mfma_f32_32x32x16_bf16 v[0:15], v[140:143], v[180:183], v[0:15]\n" \
  "ds_read_b64_tr_b16 v[112:113], v200 offset:29184\n" \
  "ds_read_b64_tr_b16 v[114:115], v200 offset:30080\n" \
  "ds_read_b64_tr_b16 v[116:117], v200 offset:29216\n" \
  "v_mfma_f32_32x32x16_bf16 v[16:31], v[144:147], v[180:183], v[16:31]\n" \
  "ds_read_b64_tr_b16 v[118:119], v200 offset:30112\n" \
  "ds_read_b64_tr_b16 v[120:121], v200 offset:29248\n" \
  "ds_read_b64_tr_b16 v[122:123], v200 offset:30144\n" \
  "v_mfma_f32_32x32x16_bf16 v[32:47], v[148:151], v[180:183], v[32:47]\n" \
  "ds_read_b64_tr_b16 v[124:125], v200 offset:29280\n" \
  "ds_read_b64_tr_b16 v[126:127], v200 offset:30176\n" \
  "ds_read_b64_tr_b16 v[128:129], v200 offset:29312\n" \
  "v_mfma_f32_32x32x16_bf16 v[48:63], v[152:155], v[180:183], v[48:63]\n" \
  "ds_read_b64_tr_b16 v[130:131], v200 offset:30208\n" \
  "ds_read_b64_tr_b16 v[132:133], v200 offset:29344\n" \
  "ds_read_b64_tr_b16 v[134:135], v200 offset:30240\n" \
  "s_mov_b64 exec, %[dmask]\n" \
  "s_add_u32 m0, %[m0b], 14592\n" \
  "s_nop 0\n" \
  "global_load_lds_dwordx4 v202, s[40:41]\n" \
  "s_mov_b64 exec, -1\n" \
  "v_mfma_f32_32x32x16_bf16 v[64:79], v[156:159], v[180:183], v[64:79]\n" \
  "ds_read_b64_tr_b16 v[136:137], v200 offset:29376\n" \
  "ds_read_b64_tr_b16 v[138:139], v200 offset:30272\n" \
  "s_mov_b64 exec, %[dmask]\n" \
  "s_add_u32 m0, %[m0b], 15488\n" \
  "s_nop 0\n" \
  "global_load_lds_dwordx4 v203, s[40:41]\n" \
  "s_mov_b64 exec, -1\n" \
  "v_mfma_f32_32x32x16_bf16 v[80:95], v[160:163], v[180:183], v[80:95]\n" \
  "s_mov_b64 exec, %[dmask]\n" \
  "s_add_u32 m0, %[m0b], 16384\n" \
  "s_nop 0\n" \
  "global_load_lds_dwordx4 v204, s[40:41]\n" \
  "s_mov_b64 exec, -1\n" \
  "v_mfma_f32_32x32x16_bf16 v[96:111], v[164:167], v[180:183], v[96:111]\n" \
  "s_mov_b64 exec, %[dmask]\n" \
  "s_add_u32 m0, %[m0b], 17280\n" \
  "s_nop 0\n" \
  "global_load_lds_dwordx4 v205, s[40:41]\n" \
  "s_mov_b64 exec, -1\n" \
  "s_add_u32 s40, s40, 114688\n" \
  "s_addc_u32 s41, s41, 0\n" \
  "global_load_dwordx4 v[180:183], v201, s[42:43]\n" \
  "s_add_u32 s42, s42, 1024\n" \
  "s_addc_u32 s43, s43, 0\n" \
  "s_waitcnt lgkmcnt(0)\n" \
  "v_mfma_f32_32x32x16_bf16 v[0:15], v[112:115], v[184:187], v[0:15]\n" \
  "ds_read_b64_tr_b16 v[140:141], v200 offset:32768\n" \
  "ds_read_b64_tr_b16 v[142:143], v200 offset:33664\n" \
  "ds_read_b64_tr_b16 v[144:145], v200 offset:32800\n" \
  "v_mfma_f32_32x32x16_bf16 v[16:31], v[116:119], v[184:187], v[16:31]\n" \
  "ds_read_b64_tr_b16 v[146:147], v200 offset:33696\n" \
  "ds_read_b64_tr_b16 v[148:149], v200 offset:32832\n" \
  "ds_read_b64_tr_b16 v[150:151], v200 offset:33728\n" \
  "v_mfma_f32_32x32x16_bf16 v[32:47], v[120:123], v[184:187], v[32:47]\n" \
  "ds_read_b64_tr_b16 v[152:153], v200 offset:32864\n" \
  "ds_read_b64_tr_b16 v[154:155], v200 offset:33760\n" \
  "ds_read_b64_tr_b16 v[156:157], v200 offset:32896\n" \
  "v_mfma_f32_32x32x16_bf16 v[48:63], v[124:127], v[184:187], v[48:63]\n" \
  "ds_read_b64_tr_b16 v[158:159], v200 offset:33792\n" \
  "ds_read_b64_tr_b16 v[160:161], v200 offset:32928\n" \
  "ds_read_b64_tr_b16 v[162:163], v200 offset:33824\n" \
  "v_mfma_f32_32x32x16_bf16 v[64:79], v[128:131], v[184:187], v[64:79]\n" \
  "ds_read_b64_tr_b16 v[164:165], v200 offset:32960\n" \
  "ds_read_b64_tr_b16 v[166:167], v200 offset:33856\n" \
  "v_mfma_f32_32x32x16_bf16 v[80:95], v[132:135], v[184:187], v[80:95]\n" \
  "v_mfma_f32_32x32x16_bf16 v[96:111], v[136:139], v[184:187], v[96:111]\n" \
  "global_load_dwordx4 v[184:187], v201, s[42:43]\n" \
  "s_add_u32 s42, s42, 1024\n" \
  "s_addc_u32 s43, s43, 0\n" \
  "s_waitcnt lgkmcnt(0)\n" \
  "s_waitcnt vmcnt(13)\n" \
  "s_barrier\n" \
  "v_mfma_f32_32x32x16_bf16 v[0:15], v[140:143], v[188:191], v[0:15]\n" \
  "ds_read_b64_tr_b16 v[112:113], v200 offset:43776\n" \
  "ds_read_b64_tr_b16 v[114:115], v200 offset:44672\n" \
  "ds_read_b64_tr_b16 v[116:117], v200 offset:43808\n" \
  "v_mfma_f32_32x32x16_bf16 v[16:31], v[144:147], v[188:191], v[16:31]\n" \
  "ds_read_b64_tr_b16 v[118:119], v200 offset:44704\n" \
  "ds_read_b64_tr_b16 v[120:121], v200 offset:43840\n" \
  "ds_read_b64_tr_b16 v[122:123], v200 offset:44736\n" \
  "v_mfma_f32_32x32x16_bf16 v[32:47], v[148:151], v[188:191], v[32:47]\n" \
  "ds_read_b64_tr_b16 v[124:125], v200 offset:43872\n" \
  "ds_read_b64_tr_b16 v[126:127], v200 offset:44768\n" \
  "ds_read_b64_tr_b16 v[128:129], v200 offset:43904\n" \
  "v_mfma_f32_32x32x16_bf16 v[48:63], v[152:155], v[188:191], v[48:63]\n" \
  "ds_read_b64_tr_b16 v[130:131], v200 offset:44800\n" \
  "ds_read_b64_tr_b16 v[132:133], v200 offset:43936\n" \
  "ds_read_b64_tr_b16 v[134:135], v200 offset:44832\n" \
  "s_mov_b64 exec, %[dmask]\n" \
  "s_add_u32 m0, %[m0b], 29184\n" \
  "s_nop 0\n" \
  "global_load_lds_dwordx4 v202, s[40:41]\n" \
  "s_mov_b64 exec, -1\n" \
  "v_mfma_f32_32x32x16_bf16 v[64:79], v[156:159], v[188:191], v[64:79]\n" \
  "ds_read_b64_tr_b16 v[136:137], v200 offset:43968\n" \
  "ds_read_b64_tr_b16 v[138:139], v200 offset:44864\n" \
  "s_mov_b64 exec, %[dmask]\n" \
  "s_add_u32 m0, %[m0b], 30080\n" \
  "s_nop 0\n" \
  "global_load_lds_dwordx4 v203, s[40:41]\n" \
  "s_mov_b64 exec, -1\n" \
  "v_mfma_f32_32x32x16_bf16 v[80:95], v[160:163], v[188:191], v[80:95]\n" \
  "s_mov_b64 exec, %[dmask]\n" \
  "s_add_u32 m0, %[m0b], 30976\n" \
  "s_nop 0\n" \
  "global_load_lds_dwordx4 v204, s[40:41]\n" \
  "s_mov_b64 exec, -1\n" \
  "v_mfma_f32_32x32x16_bf16 v[96:111], v[164:167], v[188:191], v[96:111]\n" \
  "s_mov_b64 exec, %[dmask]\n" \
  "s_add_u32 m0, %[m0b], 31872\n" \
  "s_nop 0\n" \
  "global_load_lds_dwordx4 v205, s[40:41]\n" \
  "s_mov_b64 exec, -1\n" \
  "s_add_u32 s40, s40, 114688\n" \
  "s_addc_u32 s41, s41, 0\n" \
  "global_load_dwordx4 v[188:191], v201, s[42:43]\n" \
  "s_add_u32 s42, s42, 1024\n" \
  "s_addc_u32 s43, s43, 0\n" \
  "s_waitcnt lgkmcnt(0)\n" \
  "v_mfma_f32_32x32x16_bf16 v[0:15], v[112:115], v[192:195], v[0:15]\n" \
  "ds_read_b64_tr_b16 v[140:141], v200 offset:47360\n" \
  "ds_read_b64_tr_b16 v[142:143], v200 offset:48256\n" \
  "ds_read_b64_tr_b16 v[144:145], v200 offset:47392\n" \
  "v_mfma_f32_32x32x16_bf16 v[16:31], v[116:119], v[192:195], v[16:31]\n" \
  "ds_read_b64_tr_b16 v[146:147], v200 offset:48288\n" \
  "ds_read_b64_tr_b16 v[148:149], v200 offset:47424\n" \
  "ds_read_b64_tr_b16 v[150:151], v200 offset:48320\n" \
  "v_mfma_f32_32x32x16_bf16 v[32:47], v[120:123], v[192:195], v[32:47]\n" \
  "ds_read_b64_tr_b16 v[152:153], v200 offset:47456\n" \
  "ds_read_b64_tr_b16 v[154:155], v200 offset:48352\n" \
  "ds_read_b64_tr_b16 v[156:157], v200 offset:47488\n" \
  "v_mfma_f32_32x32x16_bf16 v[48:63], v[124:127], v[192:195], v[48:63]\n" \
  "ds_read_b64_tr_b16 v[158:159], v200 offset:48384\n" \
  "ds_read_b64_tr_b16 v[160:161], v200 offset:47520\n" \
  "ds_read_b64_tr_b16 v[162:163], v200 offset:48416\n" \
  "v_mfma_f32_32x32x16_bf16 v[64:79], v[128:131], v[192:195], v[64:79]\n" \
  "ds_read_b64_tr_b16 v[164:165], v200 offset:47552\n" \
  "ds_read_b64_tr_b16 v[166:167], v200 offset:48448\n" \
  "v_mfma_f32_32x32x16_bf16 v[80:95], v[132:135], v[192:195], v[80:95]\n" \
  "v_mfma_f32_32x32x16_bf16 v[96:111], v[136:139], v[192:195], v[96:111]\n" \
  "global_load_dwordx4 v[192:195], v201, s[42:43]\n" \
  "s_add_u32 s42, s42, 1024\n" \
  "s_addc_u32 s43, s43, 0\n" \
  "s_waitcnt lgkmcnt(0)\n" \
  "s_waitcnt vmcnt(13)\n" \
  "s_barrier\n" \
  "v_mfma_f32_32x32x16_bf16 v[0:15], v[140:143], v[196:199], v[0:15]\n" \
  "ds_read_b64_tr_b16 v[112:113], v200 offset:0\n" \
  "ds_read_b64_tr_b16 v[114:115], v200 offset:896\n" \
  "ds_read_b64_tr_b16 v[116:117], v200 offset:32\n" \
  "v_mfma_f32_32x32x16_bf16 v[16:31], v[144:147], v[196:199], v[16:31]\n" \
  "ds_read_b64_tr_b16 v[118:119], v200 offset:928\n" \
  "ds_read_b64_tr_b16 v[120:121], v200 offset:64\n" \
  "ds_read_b64_tr_b16 v[122:123], v200 offset:960\n" \
  "v_mfma_f32_32x32x16_bf16 v[32:47], v[148:151], v[196:199], v[32:47]\n" \
  "ds_read_b64_tr_b16 v[124:125], v200 offset:96\n" \
  "ds_read_b64_tr_b16 v[126:127], v200 offset:992\n" \
  "ds_read_b64_tr_b16 v[128:129], v200 offset:128\n" \
  "v_mfma_f32_32x32x16_bf16 v[48:63], v[152:155], v[196:199], v[48:63]\n" \
  "ds_read_b64_tr_b16 v[130:131], v200 offset:1024\n" \
  "ds_read_b64_tr_b16 v[132:133], v200 offset:160\n" \
  "ds_read_b64_tr_b16 v[134:135], v200 offset:1056\n" \
  "s_mov_b64 exec, %[dmask]\n" \
  "s_add_u32 m0, %[m0b], 43776\n" \
  "s_nop 0\n" \
  "global_load_lds_dwordx4 v202, s[40:41]\n" \
  "s_mov_b64 exec, -1\n" \
  "v_mfma_f32_32x32x16_bf16 v[64:79], v[156:159], v[196:199], v[64:79]\n" \
  "ds_read_b64_tr_b16 v[136:137], v200 offset:192\n" \
  "ds_read_b64_tr_b16 v[138:139], v200 offset:1088\n" \
  "s_mov_b64 exec, %[dmask]\n" \
  "s_add_u32 m0, %[m0b], 44672\n" \
  "s_nop 0\n" \
  "global_load_lds_dwordx4 v203, s[40:41]\n" \
  "s_mov_b64 exec, -1\n" \
  "v_mfma_f32_32x32x16_bf16 v[80:95], v[160:163], v[196:199], v[80:95]\n" \
  "s_mov_b64 exec, %[dmask]\n" \
  "s_add_u32 m0, %[m0b], 45568\n" \
  "s_nop 0\n" \
  "global_load_lds_dwordx4 v204, s[40:41]\n" \
  "s_mov_b64 exec, -1\n" \
  "v_mfma_f32_32x32x16_bf16 v[96:111], v[164:167], v[196:199], v[96:111]\n" \
  "s_mov_b64 exec, %[dmask]\n" \
  "s_add_u32 m0, %[m0b], 46464\n" \
  "s_nop 0\n" \
  "global_load_lds_dwordx4 v205, s[40:41]\n" \
  "s_mov_b64 exec, -1\n" \
  "s_add_u32 s40, s40, 114688\n" \
  "s_addc_u32 s41, s41, 0\n" \
  "global_load_dwordx4 v[196:199], v201, s[42:43]\n" \
  "s_add_u32 s42, s42, 1024\n" \
  "s_addc_u32 s43, s43, 0\n" \
  "s_waitcnt lgkmcnt(0)\n" \
  "v_mfma_f32_32x32x16_bf16 v[0:15], v[112:115], v[168:171], v[0:15]\n" \
  "ds_read_b64_tr_b16 v[140:141], v200 offset:3584\n" \
  "ds_read_b64_tr_b16 v[142:143], v200 offset:4480\n" \
  "ds_read_b64_tr_b16 v[144:145], v200 offset:3616\n" \
  "v_mfma_f32_32x32x16_bf16 v[16:31], v[116:119], v[168:171], v[16:31]\n" \
  "ds_read_b64_tr_b16 v[146:147], v200 offset:4512\n" \
  "ds_read_b64_tr_b16 v[148:149], v200 offset:3648\n" \
  "ds_read_b64_tr_b16 v[150:151], v200 offset:4544\n" \
  "v_mfma_f32_32x32x16_bf16 v[32:47], v[120:123], v[168:171], v[32:47]\n" \
  "ds_read_b64_tr_b16 v[152:153], v200 offset:3680\n" \
  "ds_read_b64_tr_b16 v[154:155], v200 offset:4576\n" \
  "ds_read_b64_tr_b16 v[156:157], v200 offset:3712\n" \
  "v_mfma_f32_32x32x16_bf16 v[48:63], v[124:127], v[168:171], v[48:63]\n" \
  "ds_read_b64_tr_b16 v[158:159], v200 offset:4608\n" \
  "ds_read_b64_tr_b16 v[160:161], v200 offset:3744\n" \
  "ds_read_b64_tr_b16 v[162:163], v200 offset:4640\n" \
  "v_mfma_f32_32x32x16_bf16 v[64:79], v[128:131], v[168:171], v[64:79]\n" \
  "ds_read_b64_tr_b16 v[164:165], v200 offset:3776\n" \
  "ds_read_b64_tr_b16 v[166:167], v200 offset:4672\n" \
  "v_mfma_f32_32x32x16_bf16 v[80:95], v[132:135], v[168:171], v[80:95]\n" \
  "v_mfma_f32_32x32x16_bf16 v[96:111], v[136:139], v[168:171], v[96:111]\n" \
  "global_load_dwordx4 v[168:171], v201, s[42:43]\n" \
  "s_add_u32 s42, s42, 1024\n" \
  "s_addc_u32 s43, s43, 0\n" \
  "s_waitcnt lgkmcnt(0)\n" \
  "s_waitcnt vmcnt(13)\n" \
  "s_barrier\n" \
  "v_mfma_f32_32x32x16_bf16 v[0:15], v[140:143], v[172:175], v[0:15]\n" \
  "ds_read_b64_tr_b16 v[112:113], v200 offset:14592\n" \
  "ds_read_b64_tr_b16 v[114:115], v200 offset:15488\n" \
  "ds_read_b64_tr_b16 v[116:117], v200 offset:14624\n" \
  "v_mfma_f32_32x32x16_bf16 v[16:31], v[144:147], v[172:175], v[16:31]\n" \
  "ds_read_b64_tr_b16 v[118:119], v200 offset:15520\n" \
  "ds_read_b64_tr_b16 v[120:121], v200 offset:14656\n" \
  "ds_read_b64_tr_b16 v[122:123], v200 offset:15552\n" \
  "v_mfma_f32_32x32x16_bf16 v[32:47], v[148:151], v[172:175], v[32:47]\n" \
  "ds_read_b64_tr_b16 v[124:125], v200 offset:14688\n" \
  "ds_read_b64_tr_b16 v[126:127], v200 offset:15584\n" \
  "ds_read_b64_tr_b16 v[128:129], v200 offset:14720\n" \
  "v_mfma_f32_32x32x16_bf16 v[48:63], v[152:155], v[172:175], v[48:63]\n" \
  "ds_read_b64_tr_b16 v[130:131], v200 offset:15616\n" \
  "ds_read_b64_tr_b16 v[132:133], v200 offset:14752\n" \
  "ds_read_b64_tr_b16 v[134:135], v200 offset:15648\n" \
  "s_mov_b64 exec, %[dmask]\n" \
  "s_add_u32 m0, %[m0b], 0\n" \
  "s_nop 0\n" \
  "global_load_lds_dwordx4 v202, s[40:41]\n" \
  "s_mov_b64 exec, -1\n" \
  "v_mfma_f32_32x32x16_bf16 v[64:79], v[156:159], v[172:175], v[64:79]\n" \
  "ds_read_b64_tr_b16 v[136:137], v200 offset:14784\n" \
  "ds_read_b64_tr_b16 v[138:139], v200 offset:15680\n" \
  "s_mov_b64 exec, %[dmask]\n" \
  "s_add_u32 m0, %[m0b], 896\n" \
  "s_nop 0\n" \
  "global_load_lds_dwordx4 v203, s[40:41]\n" \
  "s_mov_b64 exec, -1\n" \
  "v_mfma_f32_32x32x16_bf16 v[80:95], v[160:163], v[172:175], v[80:95]\n" \
  "s_mov_b64 exec, %[dmask]\n" \
  "s_add_u32 m0, %[m0b], 1792\n" \
  "s_nop 0\n" \
  "global_load_lds_dwordx4 v204, s[40:41]\n" \
  "s_mov_b64 exec, -1\n" \
  "v_mfma_f32_32x32x16_bf16 v[96:111], v[164:167], v[172:175], v[96:111]\n" \
  "s_mov_b64 exec, %[dmask]\n" \
  "s_add_u32 m0, %[m0b], 2688\n" \
  "s_nop 0\n" \
  "global_load_lds_dwordx4 v205, s[40:41]\n" \
  "s_mov_b64 exec, -1\n" \
  "s_add_u32 s40, s40, 114688\n" \
  "s_addc_u32 s41, s41, 0\n" \
  "global_load_dwordx4 v[172:175], v201, s[42:43]\n" \
  "s_add_u32 s42, s42, 1024\n" \
  "s_addc_u32 s43, s43, 0\n" \
  "s_waitcnt lgkmcnt(0)\n" \
  "v_mfma_f32_32x32x16_bf16 v[0:15], v[112:115], v[176:179], v[0:15]\n" \
  "ds_read_b64_tr_b16 v[140:141], v200 offset:18176\n" \
  "ds_read_b64_tr_b16 v[142:143], v200 offset:19072\n" \
  "ds_read_b64_tr_b16 v[144:145], v200 offset:18208\n" \
  "v_mfma_f32_32x32x16_bf16 v[16:31], v[116:119], v[176:179], v[16:31]\n" \
  "ds_read_b64_tr_b16 v[146:147], v200 offset:19104\n" \
  "ds_read_b64_tr_b16 v[148:149], v200 offset:18240\n" \
  "ds_read_b64_tr_b16 v[150:151], v200 offset:19136\n" \
  "v_mfma_f32_32x32x16_bf16 v[32:47], v[120:123], v[176:179], v[32:47]\n" \
  "ds_read_b64_tr_b16 v[152:153], v200 offset:18272\n" \
  "ds_read_b64_tr_b16 v[154:155], v200 offset:19168\n" \
  "ds_read_b64_tr_b16 v[156:157], v200 offset:18304\n" \
  "v_mfma_f32_32x32x16_bf16 v[48:63], v[124:127], v[176:179], v[48:63]\n" \
  "ds_read_b64_tr_b16 v[158:159], v200 offset:19200\n" \
  "ds_read_b64_tr_b16 v[160:161], v200 offset:18336\n" \
  "ds_read_b64_tr_b16 v[162:163], v200 offset:19232\n" \
  "v_mfma_f32_32x32x16_bf16 v[64:79], v[128:131], v[176:179], v[64:79]\n" \
  "ds_read_b64_tr_b16 v[164:165], v200 offset:18368\n" \
  "ds_read_b64_tr_b16 v[166:167], v200 offset:19264\n" \
  "v_mfma_f32_32x32x16_bf16 v[80:95], v[132:135], v[176:179], v[80:95]\n" \
  "v_mfma_f32_32x32x16_bf16 v[96:111], v[136:139], v[176:179], v[96:111]\n" \
  "global_load_dwordx4 v[176:179], v201, s[42:43]\n" \
  "s_add_u32 s42, s42, 1024\n" \
  "s_addc_u32 s43, s43, 0\n" \
  "s_waitcnt lgkmcnt(0)\n" \
  "s_waitcnt vmcnt(13)\n" \
  "s_barrier\n" \
  "v_mfma_f32_32x32x16_bf16 v[0:15], v[140:143], v[180:183], v[0:15]\n" \
  "ds_read_b64_tr_b16 v[112:113], v200 offset:29184\n" \
  "ds_read_b64_tr_b16 v[114:115], v200 offset:30080\n" \
  "ds_read_b64_tr_b16 v[116:117], v200 offset:29216\n" \
  "v_mfma_f32_32x32x16_bf16 v[16:31], v[144:147], v[180:183], v[16:31]\n" \
  "ds_read_b64_tr_b16 v[118:119], v200 offset:30112\n" \
  "ds_read_b64_tr_b16 v[120:121], v200 offset:29248\n" \
  "ds_read_b64_tr_b16 v[122:123], v200 offset:30144\n" \
  "v_mfma_f32_32x32x16_bf16 v[32:47], v[148:151], v[180:183], v[32:47]\n" \
  "ds_read_b64_tr_b16 v[124:125], v200 offset:29280\n" \
  "ds_read_b64_tr_b16 v[126:127], v200 offset:30176\n" \
  "ds_read_b64_tr_b16 v[128:129], v200 offset:29312\n" \
  "v_mfma_f32_32x32x16_bf16 v[48:63], v[152:155], v[180:183], v[48:63]\n" \
  "ds_read_b64_tr_b16 v[130:131], v200 offset:30208\n" \
  "ds_read_b64_tr_b16 v[132:133], v200 offset:29344\n" \
  "ds_read_b64_tr_b16 v[134:135], v200 offset:30240\n" \
  "s_mov_b64 exec, %[dmask]\n" \
  "s_add_u32 m0, %[m0b], 14592\n" \
  "s_nop 0\n" \
  "global_load_lds_dwordx4 v202, s[40:41]\n" \
  "s_mov_b64 exec, -1\n" \
  "v_mfma_f32_32x32x16_bf16 v[64:79], v[156:159], v[180:183], v[64:79]\n" \
  "ds_read_b64_tr_b16 v[136:137], v200 offset:29376\n" \
  "ds_read_b64_tr_b16 v[138:139], v200 offset:30272\n" \
  "s_mov_b64 exec, %[dmask]\n" \
  "s_add_u32 m0, %[m0b], 15488\n" \
  "s_nop 0\n" \
  "global_load_lds_dwordx4 v203, s[40:41]\n" \
  "s_mov_b64 exec, -1\n" \
  "v_mfma_f32_32x32x16_bf16 v[80:95], v[160:163], v[180:183], v[80:95]\n" \
  "s_mov_b64 exec, %[dmask]\n" \
  "s_add_u32 m0, %[m0b], 16384\n" \
  "s_nop 0\n" \
  "global_load_lds_dwordx4 v204, s[40:41]\n" \
  "s_mov_b64 exec, -1\n" \
  "v_mfma_f32_32x32x16_bf16 v[96:111], v[164:167], v[180:183], v[96:111]\n" \
  "s_mov_b64 exec, %[dmask]\n" \
  "s_add_u32 m0, %[m0b], 17280\n" \
  "s_nop 0\n" \
  "global_load_lds_dwordx4 v205, s[40:41]\n" \
  "s_mov_b64 exec, -1\n" \
  "s_add_u32 s40, s40, 114688\n" \
  "s_addc_u32 s41, s41, 0\n" \
  "global_load_dwordx4 v[180:183], v201, s[42:43]\n" \
  "s_add_u32 s42, s42, 1024\n" \
  "s_addc_u32 s43, s43, 0\n" \
  "s_waitcnt lgkmcnt(0)\n" \
  "v_mfma_f32_32x32x16_bf16 v[0:15], v[112:115], v[184:187], v[0:15]\n" \
  "ds_read_b64_tr_b16 v[140:141], v200 offset:32768\n" \
  "ds_read_b64_tr_b16 v[142:143], v200 offset:33664\n" \
  "ds_read_b64_tr_b16 v[144:145], v200 offset:32800\n" \
  "v_mfma_f32_32x32x16_bf16 v[16:31], v[116:119], v[184:187], v[16:31]\n" \
  "ds_read_b64_tr_b16 v[146:147], v200 offset:33696\n" \
  "ds_read_b64_tr_b16 v[148:149], v200 offset:32832\n" \
  "ds_read_b64_tr_b16 v[150:151], v200 offset:33728\n" \
  "v_mfma_f32_32x32x16_bf16 v[32:47], v[120:123], v[184:187], v[32:47]\n" \
  "ds_read_b64_tr_b16 v[152:153], v200 offset:32864\n" \
  "ds_read_b64_tr_b16 v[154:155], v200 offset:33760\n" \
  "ds_read_b64_tr_b16 v[156:157], v200 offset:32896\n" \
  "v_mfma_f32_32x32x16_bf16 v[48:63], v[124:127], v[184:187], v[48:63]\n" \
  "ds_read_b64_tr_b16 v[158:159], v200 offset:33792\n" \
  "ds_read_b64_tr_b16 v[160:161], v200 offset:32928\n" \
  "ds_read_b64_tr_b16 v[162:163], v200 offset:33824\n" \
  "v_mfma_f32_32x32x16_bf16 v[64:79], v[128:131], v[184:187], v[64:79]\n" \
  "ds_read_b64_tr_b16 v[164:165], v200 offset:32960\n" \
  "ds_read_b64_tr_b16 v[166:167], v200 offset:33856\n" \
  "v_mfma_f32_32x32x16_bf16 v[80:95], v[132:135], v[184:187], v[80:95]\n" \
  "v_mfma_f32_32x32x16_bf16 v[96:111], v[136:139], v[184:187], v[96:111]\n" \
  "global_load_dwordx4 v[184:187], v201, s[42:43]\n" \
  "s_add_u32 s42, s42, 1024\n" \
  "s_addc_u32 s43, s43, 0\n" \
  "s_waitcnt lgkmcnt(0)\n" \
  "s_waitcnt vmcnt(13)\n" \
  "s_barrier\n" \
  "v_mfma_f32_32x32x16_bf16 v[0:15], v[140:143], v[188:191], v[0:15]\n" \
  "ds_read_b64_tr_b16 v[112:113], v200 offset:43776\n" \
  "ds_read_b64_tr_b16 v[114:115], v200 offset:44672\n" \
  "ds_read_b64_tr_b16 v[116:117], v200 offset:43808\n" \
  "v_mfma_f32_32x32x16_bf16 v[16:31], v[144:147], v[188:191], v[16:31]\n" \
  "ds_read_b64_tr_b16 v[118:119], v200 offset:44704\n" \
  "ds_read_b64_tr_b16 v[120:121], v200 offset:43840\n" \
  "ds_read_b64_tr_b16 v[122:123], v200 offset:44736\n" \
  "v_mfma_f32_32x32x16_bf16 v[32:47], v[148:151], v[188:191], v[32:47]\n" \
  "ds_read_b64_tr_b16 v[124:125], v200 offset:43872\n" \
  "ds_read_b64_tr_b16 v[126:127], v200 offset:44768\n" \
  "ds_read_b64_tr_b16 v[128:129], v200 offset:43904\n" \
  "v_mfma_f32_32x32x16_bf16 v[48:63], v[152:155], v[188:191], v[48:63]\n" \
  "ds_read_b64_tr_b16 v[130:131], v200 offset:44800\n" \
  "ds_read_b64_tr_b16 v[132:133], v200 offset:43936\n" \
  "ds_read_b64_tr_b16 v[134:135], v200 offset:44832\n" \
  "s_mov_b64 exec, %[dmask]\n" \
  "s_add_u32 m0, %[m0b], 29184\n" \
  "s_nop 0\n" \
  "global_load_lds_dwordx4 v202, s[40:41]\n" \
  "s_mov_b64 exec, -1\n" \
  "v_mfma_f32_32x32x16_bf16 v[64:79], v[156:159], v[188:191], v[64:79]\n" \
  "ds_read_b64_tr_b16 v[136:137], v200 offset:43968\n" \
  "ds_read_b64_tr_b16 v[138:139], v200 offset:44864\n" \
  "s_mov_b64 exec, %[dmask]\n" \
  "s_add_u32 m0, %[m0b], 30080\n" \
  "s_nop 0\n" \
  "global_load_lds_dwordx4 v203, s[40:41]\n" \
  "s_mov_b64 exec, -1\n" \
  "v_mfma_f32_32x32x16_bf16 v[80:95], v[160:163], v[188:191], v[80:95]\n" \
  "s_mov_b64 exec, %[dmask]\n" \
  "s_add_u32 m0, %[m0b], 30976\n" \
  "s_nop 0\n" \
  "global_load_lds_dwordx4 v204, s[40:41]\n" \
  "s_mov_b64 exec, -1\n" \
  "v_mfma_f32_32x32x16_bf16 v[96:111], v[164:167], v[188:191], v[96:111]\n" \
  "s_mov_b64 exec, %[dmask]\n" \
  "s_add_u32 m0, %[m0b], 31872\n" \
  "s_nop 0\n" \
  "global_load_lds_dwordx4 v205, s[40:41]\n" \
  "s_mov_b64 exec, -1\n" \
  "s_add_u32 s40, s40, 114688\n" \
  "s_addc_u32 s41, s41, 0\n" \
  "global_load_dwordx4 v[188:191], v201, s[42:43]\n" \
  "s_add_u32 s42, s42, 1024\n" \
  "s_addc_u32 s43, s43, 0\n" \
  "s_waitcnt lgkmcnt(0)\n" \
  "v_mfma_f32_32x32x16_bf16 v[0:15], v[112:115], v[192:195], v[0:15]\n" \
  "ds_read_b64_tr_b16 v[140:141], v200 offset:47360\n" \
  "ds_read_b64_tr_b16 v[142:143], v200 offset:48256\n" \
  "ds_read_b64_tr_b16 v[144:145], v200 offset:47392\n" \
  "v_mfma_f32_32x32x16_bf16 v[16:31], v[116:119], v[192:195], v[16:31]\n" \
  "ds_read_b64_tr_b16 v[146:147], v200 offset:48288\n" \
  "ds_read_b64_tr_b16 v[148:149], v200 offset:47424\n" \
  "ds_read_b64_tr_b16 v[150:151], v200 offset:48320\n" \
  "v_mfma_f32_32x32x16_bf16 v[32:47], v[120:123], v[192:195], v[32:47]\n" \
  "ds_read_b64_tr_b16 v[152:153], v200 offset:47456\n" \
  "ds_read_b64_tr_b16 v[154:155], v200 offset:48352\n" \
  "ds_read_b64_tr_b16 v[156:157], v200 offset:47488\n" \
  "v_mfma_f32_32x32x16_bf16 v[48:63], v[124:127], v[192:195], v[48:63]\n" \
  "ds_read_b64_tr_b16 v[158:159], v200 offset:48384\n" \
  "ds_read_b64_tr_b16 v[160:161], v200 offset:47520\n" \
  "ds_read_b64_tr_b16 v[162:163], v200 offset:48416\n" \
  "v_mfma_f32_32x32x16_bf16 v[64:79], v[128:131], v[192:195], v[64:79]\n" \
  "ds_read_b64_tr_b16 v[164:165], v200 offset:47552\n" \
  "ds_read_b64_tr_b16 v[166:167], v200 offset:48448\n" \
  "v_mfma_f32_32x32x16_bf16 v[80:95], v[132:135], v[192:195], v[80:95]\n" \
  "v_mfma_f32_32x32x16_bf16 v[96:111], v[136:139], v[192:195], v[96:111]\n" \
  "global_load_dwordx4 v[192:195], v201, s[42:43]\n" \
  "s_add_u32 s42, s42, 1024\n" \
  "s_addc_u32 s43, s43, 0\n" \
  "s_waitcnt lgkmcnt(0)\n" \
  "s_waitcnt vmcnt(13)\n" \
  "s_barrier\n" \
  "v_mfma_f32_32x32x16_bf16 v[0:15], v[140:143], v[196:199], v[0:15]\n" \
  "ds_read_b64_tr_b16 v[112:113], v200 offset:0\n" \
  "ds_read_b64_tr_b16 v[114:115], v200 offset:896\n" \
  "ds_read_b64_tr_b16 v[116:117], v200 offset:32\n" \
  "v_mfma_f32_32x32x16_bf16 v[16:31], v[144:147], v[196:199], v[16:31]\n" \
  "ds_read_b64_tr_b16 v[118:119], v200 offset:928\n" \
  "ds_read_b64_tr_b16 v[120:121], v200 offset:64\n" \
  "ds_read_b64_tr_b16 v[122:123], v200 offset:960\n" \
  "v_mfma_f32_32x32x16_bf16 v[32:47], v[148:151], v[196:199], v[32:47]\n" \
  "ds_read_b64_tr_b16 v[124:125], v200 offset:96\n" \
  "ds_read_b64_tr_b16 v[126:127], v200 offset:992\n" \
  "ds_read_b64_tr_b16 v[128:129], v200 offset:128\n" \
  "v_mfma_f32_32x32x16_bf16 v[48:63], v[152:155], v[196:199], v[48:63]\n" \
  "ds_read_b64_tr_b16 v[130:131], v200 offset:1024\n" \
  "ds_read_b64_tr_b16 v[132:133], v200 offset:160\n" \
  "ds_read_b64_tr_b16 v[134:135], v200 offset:1056\n" \
  "s_mov_b64 exec, %[dmask]\n" \
  "s_add_u32 m0, %[m0b], 43776\n" \
  "s_nop 0\n" \
  "global_load_lds_dwordx4 v202, s[40:41]\n" \
  "s_mov_b64 exec, -1\n" \
  "v_mfma_f32_32x32x16_bf16 v[64:79], v[156:159], v[196:199], v[64:79]\n" \
  "ds_read_b64_tr_b16 v[136:137], v200 offset:192\n" \
  "ds_read_b64_tr_b16 v[138:139], v200 offset:1088\n" \
  "s_mov_b64 exec, %[dmask]\n" \
  "s_add_u32 m0, %[m0b], 44672\n" \
  "s_nop 0\n" \
  "global_load_lds_dwordx4 v203, s[40:41]\n" \
  "s_mov_b64 exec, -1\n" \
  "v_mfma_f32_32x32x16_bf16 v[80:95], v[160:163], v[196:199], v[80:95]\n" \
  "s_mov_b64 exec, %[dmask]\n" \
  "s_add_u32 m0, %[m0b], 45568\n" \
  "s_nop 0\n" \
  "global_load_lds_dwordx4 v204, s[40:41]\n" \
  "s_mov_b64 exec, -1\n" \
  "v_mfma_f32_32x32x16_bf16 v[96:111], v[164:167], v[196:199], v[96:111]\n" \
  "s_mov_b64 exec, %[dmask]\n" \
  "s_add_u32 m0, %[m0b], 46464\n" \
  "s_nop 0\n" \
  "global_load_lds_dwordx4 v205, s[40:41]\n" \
  "s_mov_b64 exec, -1\n" \
  "s_add_u32 s40, s40, 114688\n" \
  "s_addc_u32 s41, s41, 0\n" \
  "global_load_dwordx4 v[196:199], v201, s[42:43]\n" \
  "s_add_u32 s42, s42, 1024\n" \
  "s_addc_u32 s43, s43, 0\n" \
  "s_waitcnt lgkmcnt(0)\n" \
  "v_mfma_f32_32x32x16_bf16 v[0:15], v[112:115], v[168:171], v[0:15]\n" \
  "ds_read_b64_tr_b16 v[140:141], v200 offset:3584\n" \
  "ds_read_b64_tr_b16 v[142:143], v200 offset:4480\n" \
  "ds_read_b64_tr_b16 v[144:145], v200 offset:3616\n" \
  "v_mfma_f32_32x32x16_bf16 v[16:31], v[116:119], v[168:171], v[16:31]\n" \
  "ds_read_b64_tr_b16 v[146:147], v200 offset:4512\n" \
  "ds_read_b64_tr_b16 v[148:149], v200 offset:3648\n" \
  "ds_read_b64_tr_b16 v[150:151], v200 offset:4544\n" \
  "v_mfma_f32_32x32x16_bf16 v[32:47], v[120:123], v[168:171], v[32:47]\n" \
  "ds_read_b64_tr_b16 v[152:153], v200 offset:3680\n" \
  "ds_read_b64_tr_b16 v[154:155], v200 offset:4576\n" \
  "ds_read_b64_tr_b16 v[156:157], v200 offset:3712\n" \
  "v_mfma_f32_32x32x16_bf16 v[48:63], v[124:127], v[168:171], v[48:63]\n" \
  "ds_read_b64_tr_b16 v[158:159], v200 offset:4608\n" \
  "ds_read_b64_tr_b16 v[160:161], v200 offset:3744\n" \
  "ds_read_b64_tr_b16 v[162:163], v200 offset:4640\n" \
  "v_mfma_f32_32x32x16_bf16 v[64:79], v[128:131], v[168:171], v[64:79]\n" \
  "ds_read_b64_tr_b16 v[164:165], v200 offset:3776\n" \
  "ds_read_b64_tr_b16 v[166:167], v200 offset:4672\n" \
  "v_mfma_f32_32x32x16_bf16 v[80:95], v[132:135], v[168:171], v[80:95]\n" \
  "v_mfma_f32_32x32x16_bf16 v[96:111], v[136:139], v[168:171], v[96:111]\n" \
  "global_load_dwordx4 v[168:171], v201, s[42:43]\n" \
  "s_add_u32 s42, s42, 1024\n" \
  "s_addc_u32 s43, s43, 0\n" \
  "s_waitcnt lgkmcnt(0)\n" \
  "s_waitcnt vmcnt(13)\n" \
  "s_barrier\n" \
  "v_mfma_f32_32x32x16_bf16 v[0:15], v[140:143], v[172:175], v[0:15]\n" \
  "ds_read_b64_tr_b16 v[112:113], v200 offset:14592\n" \
  "ds_read_b64_tr_b16 v[114:115], v200 offset:15488\n" \
  "ds_read_b64_tr_b16 v[116:117], v200 offset:14624\n" \
  "v_mfma_f32_32x32x16_bf16 v[16:31], v[144:147], v[172:175], v[16:31]\n" \
  "ds_read_b64_tr_b16 v[118:119], v200 offset:15520\n" \
  "ds_read_b64_tr_b16 v[120:121], v200 offset:14656\n" \
  "ds_read_b64_tr_b16 v[122:123], v200 offset:15552\n" \
  "v_mfma_f32_32x32x16_bf16 v[32:47], v[148:151], v[172:175], v[32:47]\n" \
  "ds_read_b64_tr_b16 v[124:125], v200 offset:14688\n" \
  "ds_read_b64_tr_b16 v[126:127], v200 offset:15584\n" \
  "ds_read_b64_tr_b16 v[128:129], v200 offset:14720\n" \
  "v_mfma_f32_32x32x16_bf16 v[48:63], v[152:155], v[172:175], v[48:63]\n" \
  "ds_read_b64_tr_b16 v[130:131], v200 offset:15616\n" \
  "ds_read_b64_tr_b16 v[132:133], v200 offset:14752\n" \
  "ds_read_b64_tr_b16 v[134:135], v200 offset:15648\n" \
  "s_mov_b64 exec, %[dmask]\n" \
  "s_add_u32 m0, %[m0b], 0\n" \
  "s_nop 0\n" \
  "global_load_lds_dwordx4 v202, s[40:41]\n" \
  "s_mov_b64 exec, -1\n" \
  "v_mfma_f32_32x32x16_bf16 v[64:79], v[156:159], v[172:175], v[64:79]\n" \
  "ds_read_b64_tr_b16 v[136:137], v200 offset:14784\n" \
  "ds_read_b64_tr_b16 v[138:139], v200 offset:15680\n" \
  "s_mov_b64 exec, %[dmask]\n" \
  "s_add_u32 m0, %[m0b], 896\n" \
  "s_nop 0\n" \
  "global_load_lds_dwordx4 v203, s[40:41]\n" \
  "s_mov_b64 exec, -1\n" \
  "v_mfma_f32_32x32x16_bf16 v[80:95], v[160:163], v[172:175], v[80:95]\n" \
  "s_mov_b64 exec, %[dmask]\n" \
  "s_add_u32 m0, %[m0b], 1792\n" \
  "s_nop 0\n" \
  "global_load_lds_dwordx4 v204, s[40:41]\n" \
  "s_mov_b64 exec, -1\n" \
  "v_mfma_f32_32x32x16_bf16 v[96:111], v[164:167], v[172:175], v[96:111]\n" \
  "s_mov_b64 exec, %[dmask]\n" \
  "s_add_u32 m0, %[m0b], 2688\n" \
  "s_nop 0\n" \
  "global_load_lds_dwordx4 v205, s[40:41]\n" \
  "s_mov_b64 exec, -1\n" \
  "s_add_u32 s40, s40, 114688\n" \
  "s_addc_u32 s41, s41, 0\n" \
  "global_load_dwordx4 v[172:175], v201, s[42:43]\n" \
  "s_add_u32 s42, s42, 1024\n" \
  "s_addc_u32 s43, s43, 0\n" \
  "s_waitcnt lgkmcnt(0)\n" \
  "v_mfma_f32_32x32x16_bf16 v[0:15], v[112:115], v[176:179], v[0:15]\n" \
  "ds_read_b64_tr_b16 v[140:141], v200 offset:18176\n" \
  "ds_read_b64_tr_b16 v[142:143], v200 offset:19072\n" \
  "ds_read_b64_tr_b16 v[144:145], v200 offset:18208\n" \
  "v_mfma_f32_32x32x16_bf16 v[16:31], v[116:119], v[176:179], v[16:31]\n" \
  "ds_read_b64_tr_b16 v[146:147], v200 offset:19104\n" \
  "ds_read_b64_tr_b16 v[148:149], v200 offset:18240\n" \
  "ds_read_b64_tr_b16 v[150:151], v200 offset:19136\n" \
  "v_mfma_f32_32x32x16_bf16 v[32:47], v[120:123], v[176:179], v[32:47]\n" \
  "ds_read_b64_tr_b16 v[152:153], v200 offset:18272\n" \
  "ds_read_b64_tr_b16 v[154:155], v200 offset:19168\n" \
  "ds_read_b64_tr_b16 v[156:157], v200 offset:18304\n" \
  "v_mfma_f32_32x32x16_bf16 v[48:63], v[124:127], v[176:179], v[48:63]\n" \
  "ds_read_b64_tr_b16 v[158:159], v200 offset:19200\n" \
  "ds_read_b64_tr_b16 v[160:161], v200 offset:18336\n" \
  "ds_read_b64_tr_b16 v[162:163], v200 offset:19232\n" \
  "v_mfma_f32_32x32x16_bf16 v[64:79], v[128:131], v[176:179], v[64:79]\n" \
  "ds_read_b64_tr_b16 v[164:165], v200 offset:18368\n" \
  "ds_read_b64_tr_b16 v[166:167], v200 offset:19264\n" \
  "v_mfma_f32_32x32x16_bf16 v[80:95], v[132:135], v[176:179], v[80:95]\n" \
  "v_mfma_f32_32x32x16_bf16 v[96:111], v[136:139], v[176:179], v[96:111]\n" \
  "global_load_dwordx4 v[176:179], v201, s[42:43]\n" \
  "s_add_u32 s42, s42, 1024\n" \
  "s_addc_u32 s43, s43, 0\n" \
  "s_waitcnt lgkmcnt(0)\n" \
  "s_waitcnt vmcnt(13)\n" \
  "s_barrier\n" \
  "v_mfma_f32_32x32x16_bf16 v[0:15], v[140:143], v[180:183], v[0:15]\n" \
  "ds_read_b64_tr_b16 v[112:113], v200 offset:29184\n" \
  "ds_read_b64_tr_b16 v[114:115], v200 offset:30080\n" \
  "ds_read_b64_tr_b16 v[116:117], v200 offset:29216\n" \
  "v_mfma_f32_32x32x16_bf16 v[16:31], v[144:147], v[180:183], v[16:31]\n" \
  "ds_read_b64_tr_b16 v[118:119], v200 offset:30112\n" \
  "ds_read_b64_tr_b16 v[120:121], v200 offset:29248\n" \
  "ds_read_b64_tr_b16 v[122:123], v200 offset:30144\n" \
  "v_mfma_f32_32x32x16_bf16 v[32:47], v[148:151], v[180:183], v[32:47]\n" \
  "ds_read_b64_tr_b16 v[124:125], v200 offset:29280\n" \
  "ds_read_b64_tr_b16 v[126:127], v200 offset:30176\n" \
  "ds_read_b64_tr_b16 v[128:129], v200 offset:29312\n" \
  "v_mfma_f32_32x32x16_bf16 v[48:63], v[152:155], v[180:183], v[48:63]\n" \
  "ds_read_b64_tr_b16 v[130:131], v200 offset:30208\n" \
  "ds_read_b64_tr_b16 v[132:133], v200 offset:29344\n" \
  "ds_read_b64_tr_b16 v[134:135], v200 offset:30240\n" \
  "s_mov_b64 exec, %[dmask]\n" \
  "s_add_u32 m0, %[m0b], 14592\n" \
  "s_nop 0\n" \
  "global_load_lds_dwordx4 v202, s[40:41]\n" \
  "s_mov_b64 exec, -1\n" \
  "v_mfma_f32_32x32x16_bf16 v[64:79], v[156:159], v[180:183], v[64:79]\n" \
  "ds_read_b64_tr_b16 v[136:137], v200 offset:29376\n" \
  "ds_read_b64_tr_b16 v[138:139], v200 offset:30272\n" \
  "s_mov_b64 exec, %[dmask]\n" \
  "s_add_u32 m0, %[m0b], 15488\n" \
  "s_nop 0\n" \
  "global_load_lds_dwordx4 v203, s[40:41]\n" \
  "s_mov_b64 exec, -1\n" \
  "v_mfma_f32_32x32x16_bf16 v[80:95], v[160:163], v[180:183], v[80:95]\n" \
  "s_mov_b64 exec, %[dmask]\n" \
  "s_add_u32 m0, %[m0b], 16384\n" \
  "s_nop 0\n" \
  "global_load_lds_dwordx4 v204, s[40:41]\n" \
  "s_mov_b64 exec, -1\n" \
  "v_mfma_f32_32x32x16_bf16 v[96:111], v[164:167], v[180:183], v[96:111]\n" \
  "s_mov_b64 exec, %[dmask]\n" \
  "s_add_u32 m0, %[m0b], 17280\n" \
  "s_nop 0\n" \
  "global_load_lds_dwordx4 v205, s[40:41]\n" \
  "s_mov_b64 exec, -1\n" \
  "s_add_u32 s40, s40, 114688\n" \
  "s_addc_u32 s41, s41, 0\n" \
  "global_load_dwordx4 v[180:183], v201, s[42:43]\n" \
  "s_add_u32 s42, s42, 1024\n" \
  "s_addc_u32 s43, s43, 0\n" \
  "s_waitcnt lgkmcnt(0)\n" \
  "v_mfma_f32_32x32x16_bf16 v[0:15], v[112:115], v[184:187], v[0:15]\n" \
  "ds_read_b64_tr_b16 v[140:141], v200 offset:32768\n" \
  "ds_read_b64_tr_b16 v[142:143], v200 offset:33664\n" \
  "ds_read_b64_tr_b16 v[144:145], v200 offset:32800\n" \
  "v_mfma_f32_32x32x16_bf16 v[16:31], v[116:119], v[184:187], v[16:31]\n" \
  "ds_read_b64_tr_b16 v[146:147], v200 offset:33696\n" \
  "ds_read_b64_tr_b16 v[148:149], v200 offset:32832\n" \
  "ds_read_b64_tr_b16 v[150:151], v200 offset:33728\n" \
  "v_mfma_f32_32x32x16_bf16 v[32:47], v[120:123], v[184:187], v[32:47]\n" \
  "ds_read_b64_tr_b16 v[152:153], v200 offset:32864\n" \
  "ds_read_b64_tr_b16 v[154:155], v200 offset:33760\n" \
  "ds_read_b64_tr_b16 v[156:157], v200 offset:32896\n" \
  "v_mfma_f32_32x32x16_bf16 v[48:63], v[124:127], v[184:187], v[48:63]\n" \
  "ds_read_b64_tr_b16 v[158:159], v200 offset:33792\n" \
  "ds_read_b64_tr_b16 v[160:161], v200 offset:32928\n" \
  "ds_read_b64_tr_b16 v[162:163], v200 offset:33824\n" \
  "v_mfma_f32_32x32x16_bf16 v[64:79], v[128:131], v[184:187], v[64:79]\n" \
  "ds_read_b64_tr_b16 v[164:165], v200 offset:32960\n" \
  "ds_read_b64_tr_b16 v[166:167], v200 offset:33856\n" \
  "v_mfma_f32_32x32x16_bf16 v[80:95], v[132:135], v[184:187], v[80:95]\n" \
  "v_mfma_f32_32x32x16_bf16 v[96:111], v[136:139], v[184:187], v[96:111]\n" \
  "global_load_dwordx4 v[184:187], v201, s[42:43]\n" \
  "s_add_u32 s42, s42, 1024\n" \
  "s_addc_u32 s43, s43, 0\n" \
  "s_waitcnt lgkmcnt(0)\n" \
  "s_waitcnt vmcnt(13)\n" \
  "s_barrier\n" \
  "v_mfma_f32_32x32x16_bf16 v[0:15], v[140:143], v[188:191], v[0:15]\n" \
  "ds_read_b64_tr_b16 v[112:113], v200 offset:43776\n" \
  "ds_read_b64_tr_b16 v[114:115], v200 offset:44672\n" \
  "ds_read_b64_tr_b16 v[116:117], v200 offset:43808\n" \
  "v_mfma_f32_32x32x16_bf16 v[16:31], v[144:147], v[188:191], v[16:31]\n" \
  "ds_read_b64_tr_b16 v[118:119], v200 offset:44704\n" \
  "ds_read_b64_tr_b16 v[120:121], v200 offset:43840\n" \
  "ds_read_b64_tr_b16 v[122:123], v200 offset:44736\n" \
  "v_mfma_f32_32x32x16_bf16 v[32:47], v[148:151], v[188:191], v[32:47]\n" \
  "ds_read_b64_tr_b16 v[124:125], v200 offset:43872\n" \
  "ds_read_b64_tr_b16 v[126:127], v200 offset:44768\n" \
  "ds_read_b64_tr_b16 v[128:129], v200 offset:43904\n" \
  "v_mfma_f32_32x32x16_bf16 v[48:63], v[152:155], v[188:191], v[48:63]\n" \
  "ds_read_b64_tr_b16 v[130:131], v200 offset:44800\n" \
  "ds_read_b64_tr_b16 v[132:133], v200 offset:43936\n" \
  "ds_read_b64_tr_b16 v[134:135], v200 offset:44832\n" \
  "s_mov_b64 exec, %[dmask]\n" \
  "s_add_u32 m0, %[m0b], 29184\n" \
  "s_nop 0\n" \
  "global_load_lds_dwordx4 v202, s[40:41]\n" \
  "s_mov_b64 exec, -1\n" \
  "v_mfma_f32_32x32x16_bf16 v[64:79], v[156:159], v[188:191], v[64:79]\n" \
  "ds_read_b64_tr_b16 v[136:137], v200 offset:43968\n" \
  "ds_read_b64_tr_b16 v[138:139], v200 offset:44864\n" \
  "s_mov_b64 exec, %[dmask]\n" \
  "s_add_u32 m0, %[m0b], 30080\n" \
  "s_nop 0\n" \
  "global_load_lds_dwordx4 v203, s[40:41]\n" \
  "s_mov_b64 exec, -1\n" \
  "v_mfma_f32_32x32x16_bf16 v[80:95], v[160:163], v[188:191], v[80:95]\n" \
  "s_mov_b64 exec, %[dmask]\n" \
  "s_add_u32 m0, %[m0b], 30976\n" \
  "s_nop 0\n" \
  "global_load_lds_dwordx4 v204, s[40:41]\n" \
  "s_mov_b64 exec, -1\n" \
  "v_mfma_f32_32x32x16_bf16 v[96:111], v[164:167], v[188:191], v[96:111]\n" \
  "s_mov_b64 exec, %[dmask]\n" \
  "s_add_u32 m0, %[m0b], 31872\n" \
  "s_nop 0\n" \
  "global_load_lds_dwordx4 v205, s[40:41]\n" \
  "s_mov_b64 exec, -1\n" \
  "s_add_u32 s40, s40, 114688\n" \
  "s_addc_u32 s41, s41, 0\n" \
  "global_load_dwordx4 v[188:191], v201, s[42:43]\n" \
  "s_add_u32 s42, s42, 1024\n" \
  "s_addc_u32 s43, s43, 0\n" \
  "s_waitcnt lgkmcnt(0)\n" \
  "v_mfma_f32_32x32x16_bf16 v[0:15], v[112:115], v[192:195], v[0:15]\n" \
  "ds_read_b64_tr_b16 v[140:141], v200 offset:47360\n" \
  "ds_read_b64_tr_b16 v[142:143], v200 offset:48256\n" \
  "ds_read_b64_tr_b16 v[144:145], v200 offset:47392\n" \
  "v_mfma_f32_32x32x16_bf16 v[16:31], v[116:119], v[192:195], v[16:31]\n" \
  "ds_read_b64_tr_b16 v[146:147], v200 offset:48288\n" \
  "ds_read_b64_tr_b16 v[148:149], v200 offset:47424\n" \
  "ds_read_b64_tr_b16 v[150:151], v200 offset:48320\n" \
  "v_mfma_f32_32x32x16_bf16 v[32:47], v[120:123], v[192:195], v[32:47]\n" \
  "ds_read_b64_tr_b16 v[152:153], v200 offset:47456\n" \
  "ds_read_b64_tr_b16 v[154:155], v200 offset:48352\n" \
  "ds_read_b64_tr_b16 v[156:157], v200 offset:47488\n" \
  "v_mfma_f32_32x32x16_bf16 v[48:63], v[124:127], v[192:195], v[48:63]\n" \
  "ds_read_b64_tr_b16 v[158:159], v200 offset:48384\n" \
  "ds_read_b64_tr_b16 v[160:161], v200 offset:47520\n" \
  "ds_read_b64_tr_b16 v[162:163], v200 offset:48416\n" \
  "v_mfma_f32_32x32x16_bf16 v[64:79], v[128:131], v[192:195], v[64:79]\n" \
  "ds_read_b64_tr_b16 v[164:165], v200 offset:47552\n" \
  "ds_read_b64_tr_b16 v[166:167], v200 offset:48448\n" \
  "v_mfma_f32_32x32x16_bf16 v[80:95], v[132:135], v[192:195], v[80:95]\n" \
  "v_mfma_f32_32x32x16_bf16 v[96:111], v[136:139], v[192:195], v[96:111]\n" \
  "global_load_dwordx4 v[192:195], v201, s[42:43]\n" \
  "s_add_u32 s42, s42, 1024\n" \
  "s_addc_u32 s43, s43, 0\n" \
  "s_waitcnt lgkmcnt(0)\n" \
  "s_waitcnt vmcnt(13)\n" \
  "s_barrier\n" \
  "v_mfma_f32_32x32x16_bf16 v[0:15], v[140:143], v[196:199], v[0:15]\n" \
  "ds_read_b64_tr_b16 v[112:113], v200 offset:0\n" \
  "ds_read_b64_tr_b16 v[114:115], v200 offset:896\n" \
  "ds_read_b64_tr_b16 v[116:117], v200 offset:32\n" \
  "v_mfma_f32_32x32x16_bf16 v[16:31], v[144:147], v[196:199], v[16:31]\n" \
  "ds_read_b64_tr_b16 v[118:119], v200 offset:928\n" \
  "ds_read_b64_tr_b16 v[120:121], v200 offset:64\n" \
  "ds_read_b64_tr_b16 v[122:123], v200 offset:960\n" \
  "v_mfma_f32_32x32x16_bf16 v[32:47], v[148:151], v[196:199], v[32:47]\n" \
  "ds_read_b64_tr_b16 v[124:125], v200 offset:96\n" \
  "ds_read_b64_tr_b16 v[126:127], v200 offset:992\n" \
  "ds_read_b64_tr_b16 v[128:129], v200 offset:128\n" \
  "v_mfma_f32_32x32x16_bf16 v[48:63], v[152:155], v[196:199], v[48:63]\n" \
  "ds_read_b64_tr_b16 v[130:131], v200 offset:1024\n" \
  "ds_read_b64_tr_b16 v[132:133], v200 offset:160\n" \
  "ds_read_b64_tr_b16 v[134:135], v200 offset:1056\n" \
  "s_mov_b64 exec, %[dmask]\n" \
  "s_add_u32 m0, %[m0b], 43776\n" \
  "s_nop 0\n" \
  "global_load_lds_dwordx4 v202, s[40:41]\n" \
  "s_mov_b64 exec, -1\n" \
  "v_mfma_f32_32x32x16_bf16 v[64:79], v[156:159], v[196:199], v[64:79]\n" \
  "ds_read_b64_tr_b16 v[136:137], v200 offset:192\n" \
  "ds_read_b64_tr_b16 v[138:139], v200 offset:1088\n" \
  "s_mov_b64 exec, %[dmask]\n" \
  "s_add_u32 m0, %[m0b], 44672\n" \
  "s_nop 0\n" \
  "global_load_lds_dwordx4 v203, s[40:41]\n" \
  "s_mov_b64 exec, -1\n" \
  "v_mfma_f32_32x32x16_bf16 v[80:95], v[160:163], v[196:199], v[80:95]\n" \
  "s_mov_b64 exec, %[dmask]\n" \
  "s_add_u32 m0, %[m0b], 45568\n" \
  "s_nop 0\n" \
  "global_load_lds_dwordx4 v204, s[40:41]\n" \
  "s_mov_b64 exec, -1\n" \
  "v_mfma_f32_32x32x16_bf16 v[96:111], v[164:167], v[196:199], v[96:111]\n" \
  "s_mov_b64 exec, %[dmask]\n" \
  "s_add_u32 m0, %[m0b], 46464\n" \
  "s_nop 0\n" \
  "global_load_lds_dwordx4 v205, s[40:41]\n" \
  "s_mov_b64 exec, -1\n" \
  "s_add_u32 s40, s40, 114688\n" \
  "s_addc_u32 s41, s41, 0\n" \
  "global_load_dwordx4 v[196:199], v201, s[42:43]\n" \
  "s_add_u32 s42, s42, 1024\n" \
  "s_addc_u32 s43, s43, 0\n" \
  "s_waitcnt lgkmcnt(0)\n" \
  "v_mfma_f32_32x32x16_bf16 v[0:15], v[112:115], v[168:171], v[0:15]\n" \
  "ds_read_b64_tr_b16 v[140:141], v200 offset:3584\n" \
  "ds_read_b64_tr_b16 v[142:143], v200 offset:4480\n" \
  "ds_read_b64_tr_b16 v[144:145], v200 offset:3616\n" \
  "v_mfma_f32_32x32x16_bf16 v[16:31], v[116:119], v[168:171], v[16:31]\n" \
  "ds_read_b64_tr_b16 v[146:147], v200 offset:4512\n" \
  "ds_read_b64_tr_b16 v[148:149], v200 offset:3648\n" \
  "ds_read_b64_tr_b16 v[150:151], v200 offset:4544\n" \
  "v_mfma_f32_32x32x16_bf16 v[32:47], v[120:123], v[168:171], v[32:47]\n" \
  "ds_read_b64_tr_b16 v[152:153], v200 offset:3680\n" \
  "ds_read_b64_tr_b16 v[154:155], v200 offset:4576\n" \
  "ds_read_b64_tr_b16 v[156:157], v200 offset:3712\n" \
  "v_mfma_f32_32x32x16_bf16 v[48:63], v[124:127], v[168:171], v[48:63]\n" \
  "ds_read_b64_tr_b16 v[158:159], v200 offset:4608\n" \
  "ds_read_b64_tr_b16 v[160:161], v200 offset:3744\n" \
  "ds_read_b64_tr_b16 v[162:163], v200 offset:4640\n" \
  "v_mfma_f32_32x32x16_bf16 v[64:79], v[128:131], v[168:171], v[64:79]\n" \
  "ds_read_b64_tr_b16 v[164:165], v200 offset:3776\n" \
  "ds_read_b64_tr_b16 v[166:167], v200 offset:4672\n" \
  "v_mfma_f32_32x32x16_bf16 v[80:95], v[132:135], v[168:171], v[80:95]\n" \
  "v_mfma_f32_32x32x16_bf16 v[96:111], v[136:139], v[168:171], v[96:111]\n" \
  "s_waitcnt lgkmcnt(0)\n" \
  "s_waitcnt vmcnt(12)\n" \
  "s_barrier\n" \
  "v_mfma_f32_32x32x16_bf16 v[0:15], v[140:143], v[172:175], v[0:15]\n" \
  "ds_read_b64_tr_b16 v[112:113], v200 offset:14592\n" \
  "ds_read_b64_tr_b16 v[114:115], v200 offset:15488\n" \
  "ds_read_b64_tr_b16 v[116:117], v200 offset:14624\n" \
  "v_mfma_f32_32x32x16_bf16 v[16:31], v[144:147], v[172:175], v[16:31]\n" \
  "ds_read_b64_tr_b16 v[118:119], v200 offset:15520\n" \
  "ds_read_b64_tr_b16 v[120:121], v200 offset:14656\n" \
  "ds_read_b64_tr_b16 v[122:123], v200 offset:15552\n" \
  "v_mfma_f32_32x32x16_bf16 v[32:47], v[148:151], v[172:175], v[32:47]\n" \
  "ds_read_b64_tr_b16 v[124:125], v200 offset:14688\n" \
  "ds_read_b64_tr_b16 v[126:127], v200 offset:15584\n" \
  "ds_read_b64_tr_b16 v[128:129], v200 offset:14720\n" \
  "v_mfma_f32_32x32x16_bf16 v[48:63], v[152:155], v[172:175], v[48:63]\n" \
  "ds_read_b64_tr_b16 v[130:131], v200 offset:15616\n" \
  "ds_read_b64_tr_b16 v[132:133], v200 offset:14752\n" \
  "ds_read_b64_tr_b16 v[134:135], v200 offset:15648\n" \
  "v_mfma_f32_32x32x16_bf16 v[64:79], v[156:159], v[172:175], v[64:79]\n" \
  "ds_read_b64_tr_b16 v[136:137], v200 offset:14784\n" \
  "ds_read_b64_tr_b16 v[138:139], v200 offset:15680\n" \
  "v_mfma_f32_32x32x16_bf16 v[80:95], v[160:163], v[172:175], v[80:95]\n" \
  "v_mfma_f32_32x32x16_bf16 v[96:111], v[164:167], v[172:175], v[96:111]\n" \
  "s_waitcnt lgkmcnt(0)\n" \
  "v_mfma_f32_32x32x16_bf16 v[0:15], v[112:115], v[176:179], v[0:15]\n" \
  "ds_read_b64_tr_b16 v[140:141], v200 offset:18176\n" \
  "ds_read_b64_tr_b16 v[142:143], v200 offset:19072\n" \
  "ds_read_b64_tr_b16 v[144:145], v200 offset:18208\n" \
  "v_mfma_f32_32x32x16_bf16 v[16:31], v[116:119], v[176:179], v[16:31]\n" \
  "ds_read_b64_tr_b16 v[146:147], v200 offset:19104\n" \
  "ds_read_b64_tr_b16 v[148:149], v200 offset:18240\n" \
  "ds_read_b64_tr_b16 v[150:151], v200 offset:19136\n" \
  "v_mfma_f32_32x32x16_bf16 v[32:47], v[120:123], v[176:179], v[32:47]\n" \
  "ds_read_b64_tr_b16 v[152:153], v200 offset:18272\n" \
  "ds_read_b64_tr_b16 v[154:155], v200 offset:19168\n" \
  "ds_read_b64_tr_b16 v[156:157], v200 offset:18304\n" \
  "v_mfma_f32_32x32x16_bf16 v[48:63], v[124:127], v[176:179], v[48:63]\n" \
  "ds_read_b64_tr_b16 v[158:159], v200 offset:19200\n" \
  "ds_read_b64_tr_b16 v[160:161], v200 offset:18336\n" \
  "ds_read_b64_tr_b16 v[162:163], v200 offset:19232\n" \
  "v_mfma_f32_32x32x16_bf16 v[64:79], v[128:131], v[176:179], v[64:79]\n" \
  "ds_read_b64_tr_b16 v[164:165], v200 offset:18368\n" \
  "ds_read_b64_tr_b16 v[166:167], v200 offset:19264\n" \
  "v_mfma_f32_32x32x16_bf16 v[80:95], v[132:135], v[176:179], v[80:95]\n" \
  "v_mfma_f32_32x32x16_bf16 v[96:111], v[136:139], v[176:179], v[96:111]\n" \
  "s_waitcnt lgkmcnt(0)\n" \
  "s_waitcnt vmcnt(6)\n" \
  "s_barrier\n" \
  "v_mfma_f32_32x32x16_bf16 v[0:15], v[140:143], v[180:183], v[0:15]\n" \
  "ds_read_b64_tr_b16 v[112:113], v200 offset:29184\n" \
  "ds_read_b64_tr_b16 v[114:115], v200 offset:30080\n" \
  "ds_read_b64_tr_b16 v[116:117], v200 offset:29216\n" \
  "v_mfma_f32_32x32x16_bf16 v[16:31], v[144:147], v[180:183], v[16:31]\n" \
  "ds_read_b64_tr_b16 v[118:119], v200 offset:30112\n" \
  "ds_read_b64_tr_b16 v[120:121], v200 offset:29248\n" \
  "ds_read_b64_tr_b16 v[122:123], v200 offset:30144\n" \
  "v_mfma_f32_32x32x16_bf16 v[32:47], v[148:151], v[180:183], v[32:47]\n" \
  "ds_read_b64_tr_b16 v[124:125], v200 offset:29280\n" \
  "ds_read_b64_tr_b16 v[126:127], v200 offset:30176\n" \
  "ds_read_b64_tr_b16 v[128:129], v200 offset:29312\n" \
  "v_mfma_f32_32x32x16_bf16 v[48:63], v[152:155], v[180:183], v[48:63]\n" \
  "ds_read_b64_tr_b16 v[130:131], v200 offset:30208\n" \
  "ds_read_b64_tr_b16 v[132:133], v200 offset:29344\n" \
  "ds_read_b64_tr_b16 v[134:135], v200 offset:30240\n" \
  "v_mfma_f32_32x32x16_bf16 v[64:79], v[156:159], v[180:183], v[64:79]\n" \
  "ds_read_b64_tr_b16 v[136:137], v200 offset:29376\n" \
  "ds_read_b64_tr_b16 v[138:139], v200 offset:30272\n" \
  "v_mfma_f32_32x32x16_bf16 v[80:95], v[160:163], v[180:183], v[80:95]\n" \
  "v_mfma_f32_32x32x16_bf16 v[96:111], v[164:167], v[180:183], v[96:111]\n" \
  "s_waitcnt lgkmcnt(0)\n" \
  "v_mfma_f32_32x32x16_bf16 v[0:15], v[112:115], v[184:187], v[0:15]\n" \
  "ds_read_b64_tr_b16 v[140:141], v200 offset:32768\n" \
  "ds_read_b64_tr_b16 v[142:143], v200 offset:33664\n" \
  "ds_read_b64_tr_b16 v[144:145], v200 offset:32800\n" \
  "v_mfma_f32_32x32x16_bf16 v[16:31], v[116:119], v[184:187], v[16:31]\n" \
  "ds_read_b64_tr_b16 v[146:147], v200 offset:33696\n" \
  "ds_read_b64_tr_b16 v[148:149], v200 offset:32832\n" \
  "ds_read_b64_tr_b16 v[150:151], v200 offset:33728\n" \
  "v_mfma_f32_32x32x16_bf16 v[32:47], v[120:123], v[184:187], v[32:47]\n" \
  "ds_read_b64_tr_b16 v[152:153], v200 offset:32864\n" \
  "ds_read_b64_tr_b16 v[154:155], v200 offset:33760\n" \
  "ds_read_b64_tr_b16 v[156:157], v200 offset:32896\n" \
  "v_mfma_f32_32x32x16_bf16 v[48:63], v[124:127], v[184:187], v[48:63]\n" \
  "ds_read_b64_tr_b16 v[158:159], v200 offset:33792\n" \
  "ds_read_b64_tr_b16 v[160:161], v200 offset:32928\n" \
  "ds_read_b64_tr_b16 v[162:163], v200 offset:33824\n" \
  "v_mfma_f32_32x32x16_bf16 v[64:79], v[128:131], v[184:187], v[64:79]\n" \
  "ds_read_b64_tr_b16 v[164:165], v200 offset:32960\n" \
  "ds_read_b64_tr_b16 v[166:167], v200 offset:33856\n" \
  "v_mfma_f32_32x32x16_bf16 v[80:95], v[132:135], v[184:187], v[80:95]\n" \
  "v_mfma_f32_32x32x16_bf16 v[96:111], v[136:139], v[184:187], v[96:111]\n" \
  "s_waitcnt lgkmcnt(0)\n" \
  "s_waitcnt vmcnt(0)\n" \
  "s_barrier\n" \
  "v_mfma_f32_32x32x16_bf16 v[0:15], v[140:143], v[188:191], v[0:15]\n" \
  "ds_read_b64_tr_b16 v[112:113], v200 offset:43776\n" \
  "ds_read_b64_tr_b16 v[114:115], v200 offset:44672\n" \
  "ds_read_b64_tr_b16 v[116:117], v200 offset:43808\n" \
  "v_mfma_f32_32x32x16_bf16 v[16:31], v[144:147], v[188:191], v[16:31]\n" \
  "ds_read_b64_tr_b16 v[118:119], v200 offset:44704\n" \
  "ds_read_b64_tr_b16 v[120:121], v200 offset:43840\n" \
  "ds_read_b64_tr_b16 v[122:123], v200 offset:44736\n" \
  "v_mfma_f32_32x32x16_bf16 v[32:47], v[148:151], v[188:191], v[32:47]\n" \
  "ds_read_b64_tr_b16 v[124:125], v200 offset:43872\n" \
  "ds_read_b64_tr_b16 v[126:127], v200 offset:44768\n" \
  "ds_read_b64_tr_b16 v[128:129], v200 offset:43904\n" \
  "v_mfma_f32_32x32x16_bf16 v[48:63], v[152:155], v[188:191], v[48:63]\n" \
  "ds_read_b64_tr_b16 v[130:131], v200 offset:44800\n" \
  "ds_read_b64_tr_b16 v[132:133], v200 offset:43936\n" \
  "ds_read_b64_tr_b16 v[134:135], v200 offset:44832\n" \
  "v_mfma_f32_32x32x16_bf16 v[64:79], v[156:159], v[188:191], v[64:79]\n" \
  "ds_read_b64_tr_b16 v[136:137], v200 offset:43968\n" \
  "ds_read_b64_tr_b16 v[138:139], v200 offset:44864\n" \
  "v_mfma_f32_32x32x16_bf16 v[80:95], v[160:163], v[188:191], v[80:95]\n" \
  "v_mfma_f32_32x32x16_bf16 v[96:111], v[164:167], v[188:191], v[96:111]\n" \
  "s_waitcnt lgkmcnt(0)\n" \
  "v_mfma_f32_32x32x16_bf16 v[0:15], v[112:115], v[192:195], v[0:15]\n" \
  "ds_read_b64_tr_b16 v[140:141], v200 offset:47360\n" \
  "ds_read_b64_tr_b16 v[142:143], v200 offset:48256\n" \
  "ds_read_b64_tr_b16 v[144:145], v200 offset:47392\n" \
  "v_mfma_f32_32x32x16_bf16 v[16:31], v[116:119], v[192:195], v[16:31]\n" \
  "ds_read_b64_tr_b16 v[146:147], v200 offset:48288\n" \
  "ds_read_b64_tr_b16 v[148:149], v200 offset:47424\n" \
  "ds_read_b64_tr_b16 v[150:151], v200 offset:48320\n" \
  "v_mfma_f32_32x32x16_bf16 v[32:47], v[120:123], v[192:195], v[32:47]\n" \
  "ds_read_b64_tr_b16 v[152:153], v200 offset:47456\n" \
  "ds_read_b64_tr_b16 v[154:155], v200 offset:48352\n" \
  "ds_read_b64_tr_b16 v[156:157], v200 offset:47488\n" \
  "v_mfma_f32_32x32x16_bf16 v[48:63], v[124:127], v[192:195], v[48:63]\n" \
  "ds_read_b64_tr_b16 v[158:159], v200 offset:48384\n" \
  "ds_read_b64_tr_b16 v[160:161], v200 offset:47520\n" \
  "ds_read_b64_tr_b16 v[162:163], v200 offset:48416\n" \
  "v_mfma_f32_32x32x16_bf16 v[64:79], v[128:131], v[192:195], v[64:79]\n" \
  "ds_read_b64_tr_b16 v[164:165], v200 offset:47552\n" \
  "ds_read_b64_tr_b16 v[166:167], v200 offset:48448\n" \
  "v_mfma_f32_32x32x16_bf16 v[80:95], v[132:135], v[192:195], v[80:95]\n" \
  "v_mfma_f32_32x32x16_bf16 v[96:111], v[136:139], v[192:195], v[96:111]\n" \
  "s_waitcnt lgkmcnt(0)\n" \
  "v_mfma_f32_32x32x16_bf16 v[0:15], v[140:143], v[196:199], v[0:15]\n" \
  "v_mfma_f32_32x32x16_bf16 v[16:31], v[144:147], v[196:199], v[16:31]\n" \
  "v_mfma_f32_32x32x16_bf16 v[32:47], v[148:151], v[196:199], v[32:47]\n" \
  "v_mfma_f32_32x32x16_bf16 v[48:63], v[152:155], v[196:199], v[48:63]\n" \
  "v_mfma_f32_32x32x16_bf16 v[64:79], v[156:159], v[196:199], v[64:79]\n" \
  "v_mfma_f32_32x32x16_bf16 v[80:95], v[160:163], v[196:199], v[80:95]\n" \
  "v_mfma_f32_32x32x16_bf16 v[96:111], v[164:167], v[196:199], v[96:111]\n" \
  "s_nop 15\n" \
  ""
#define G64_PRE_ASM \
  "s_mov_b64 s[40:41], %[wb0]\n" \
  "s_add_u32 m0, %[m0b], 0\n" \
  "s_nop 0\n" \
  "global_load_lds_dwordx4 v148, s[40:41]\n" \
  "s_add_u32 m0, %[m0b], 1024\n" \
  "s_nop 0\n" \
  "global_load_lds_dwordx4 v149, s[40:41]\n" \
  "s_add_u32 s40, s40, 2048\n" \
  "s_addc_u32 s41, s41, 0\n" \
  "s_add_u32 m0, %[m0b], 2048\n" \
  "s_nop 0\n" \
  "global_load_lds_dwordx4 v148, s[40:41]\n" \
  "s_add_u32 m0, %[m0b], 3072\n" \
  "s_nop 0\n" \
  "global_load_lds_dwordx4 v149, s[40:41]\n" \
  "s_add_u32 s40, s40, 2048\n" \
  "s_addc_u32 s41, s41, 0\n" \
  "s_add_u32 m0, %[m0b], 4096\n" \
  "s_nop 0\n" \
  "global_load_lds_dwordx4 v148, s[40:41]\n" \
  "s_add_u32 m0, %[m0b], 5120\n" \
  "s_nop 0\n" \
  "global_load_lds_dwordx4 v149, s[40:41]\n" \
  "s_add_u32 s40, s40, 2048\n" \
  "s_addc_u32 s41, s41, 0\n" \
  "s_add_u32 m0, %[m0b], 6144\n" \
  "s_nop 0\n" \
  "global_load_lds_dwordx4 v148, s[40:41]\n" \
  "s_add_u32 m0, %[m0b], 7168\n" \
  "s_nop 0\n" \
  "global_load_lds_dwordx4 v149, s[40:41]\n" \
  "s_add_u32 s40, s40, 2048\n" \
  "s_addc_u32 s41, s41, 0\n" \
  "s_add_u32 m0, %[m0b], 8192\n" \
  "s_nop 0\n" \
  "global_load_lds_dwordx4 v148, s[40:41]\n" \
  "s_add_u32 m0, %[m0b], 9216\n" \
  "s_nop 0\n" \
  "global_load_lds_dwordx4 v149, s[40:41]\n" \
  "s_add_u32 s40, s40, 2048\n" \
  "s_addc_u32 s41, s41, 0\n" \
  "s_add_u32 m0, %[m0b], 10240\n" \
  "s_nop 0\n" \
  "global_load_lds_dwordx4 v148, s[40:41]\n" \
  "s_add_u32 m0, %[m0b], 11264\n" \
  "s_nop 0\n" \
  "global_load_lds_dwordx4 v149, s[40:41]\n" \
  "s_add_u32 s40, s40, 2048\n" \
  "s_addc_u32 s41, s41, 0\n" \
  "s_add_u32 m0, %[m0b], 12288\n" \
  "s_nop 0\n" \
  "global_load_lds_dwordx4 v148, s[40:41]\n" \
  "s_add_u32 m0, %[m0b], 13312\n" \
  "s_nop 0\n" \
  "global_load_lds_dwordx4 v149, s[40:41]\n" \
  "s_add_u32 s40, s40, 2048\n" \
  "s_addc_u32 s41, s41, 0\n" \
  "s_add_u32 m0, %[m0b], 14336\n" \
  "s_nop 0\n" \
  "global_load_lds_dwordx4 v148, s[40:41]\n" \
  "s_add_u32 m0, %[m0b], 15360\n" \
  "s_nop 0\n" \
  "global_load_lds_dwordx4 v149, s[40:41]\n" \
  "s_add_u32 s40, s40, 2048\n" \
  "s_addc_u32 s41, s41, 0\n" \
  ""
#define G64_DOWN_ASM \
  "s_mov_b64 s[40:41], %[wb0]\n" \
  "s_mov_b64 s[42:43], %[ab0]\n" \
  "global_load_dwordx4 v[64:67], v146, s[42:43] sc1\n" \
  "global_load_dwordx4 v[68:71], v147, s[42:43] sc1\n" \
  "s_add_u32 s42, s42, 1024\n" \
  "s_addc_u32 s43, s43, 0\n" \
  "global_load_dwordx4 v[72:75], v146, s[42:43] sc1\n" \
  "global_load_dwordx4 v[76:79], v147, s[42:43] sc1\n" \
  "s_add_u32 s42, s42, 1024\n" \
  "s_addc_u32 s43, s43, 0\n" \
  "global_load_dwordx4 v[80:83], v146, s[42:43] sc1\n" \
  "global_load_dwordx4 v[84:87], v147, s[42:43] sc1\n" \
  "s_add_u32 s42, s42, 1024\n" \
  "s_addc_u32 s43, s43, 0\n" \
  "global_load_dwordx4 v[88:91], v146, s[42:43] sc1\n" \
  "global_load_dwordx4 v[92:95], v147, s[42:43] sc1\n" \
  "s_add_u32 s42, s42, 1024\n" \
  "s_addc_u32 s43, s43, 0\n" \
  "global_load_dwordx4 v[96:99], v146, s[42:43] sc1\n" \
  "global_load_dwordx4 v[100:103], v147, s[42:43] sc1\n" \
  "s_add_u32 s42, s42, 1024\n" \
  "s_addc_u32 s43, s43, 0\n" \
  "global_load_dwordx4 v[104:107], v146, s[42:43] sc1\n" \
  "global_load_dwordx4 v[108:111], v147, s[42:43] sc1\n" \
  "s_add_u32 s42, s42, 1024\n" \
  "s_addc_u32 s43, s43, 0\n" \
  "global_load_dwordx4 v[112:115], v146, s[42:43] sc1\n" \
  "global_load_dwordx4 v[116:119], v147, s[42:43] sc1\n" \
  "s_add_u32 s42, s42, 1024\n" \
  "s_addc_u32 s43, s43, 0\n" \
  "global_load_dwordx4 v[120:123], v146, s[42:43] sc1\n" \
  "global_load_dwordx4 v[124:127], v147, s[42:43] sc1\n" \
  "s_add_u32 s42, s42, 1024\n" \
  "s_addc_u32 s43, s43, 0\n" \
  "s_waitcnt vmcnt(14)\n" \
  "ds_read_b64_tr_b16 v[128:129], v144 offset:0\n" \
  "ds_read_b64_tr_b16 v[130:131], v144 offset:1024\n" \
  "ds_read_b64_tr_b16 v[132:133], v145 offset:0\n" \
  "ds_read_b64_tr_b16 v[134:135], v145 offset:1024\n" \
  "s_waitcnt vmcnt(12)\n" \
  "s_waitcnt lgkmcnt(0)\n" \
  "v_mfma_f32_32x32x16_bf16 v[0:15], v[128:131], v[64:67], 0\n" \
  "ds_read_b64_tr_b16 v[136:137], v144 offset:2048\n" \
  "ds_read_b64_tr_b16 v[138:139], v144 offset:3072\n" \
  "v_mfma_f32_32x32x16_bf16 v[16:31], v[132:135], v[64:67], 0\n" \
  "ds_read_b64_tr_b16 v[140:141], v145 offset:2048\n" \
  "ds_read_b64_tr_b16 v[142:143], v145 offset:3072\n" \
  "v_mfma_f32_32x32x16_bf16 v[32:47], v[128:131], v[68:71], 0\n" \
  "v_mfma_f32_32x32x16_bf16 v[48:63], v[132:135], v[68:71], 0\n" \
  "s_add_u32 m0, %[m0b], 0\n" \
  "s_nop 0\n" \
  "global_load_lds_dwordx4 v148, s[40:41]\n" \
  "s_add_u32 m0, %[m0b], 1024\n" \
  "s_nop 0\n" \
  "global_load_lds_dwordx4 v149, s[40:41]\n" \
  "s_add_u32 s40, s40, 2048\n" \
  "s_addc_u32 s41, s41, 0\n" \
  "global_load_dwordx4 v[64:67], v146, s[42:43] sc1\n" \
  "global_load_dwordx4 v[68:71], v147, s[42:43] sc1\n" \
  "s_add_u32 s42, s42, 1024\n" \
  "s_addc_u32 s43, s43, 0\n" \
  "s_waitcnt vmcnt(14)\n" \
  "s_waitcnt lgkmcnt(0)\n" \
  "v_mfma_f32_32x32x16_bf16 v[0:15], v[136:139], v[72:75], v[0:15]\n" \
  "ds_read_b64_tr_b16 v[128:129], v144 offset:4096\n" \
  "ds_read_b64_tr_b16 v[130:131], v144 offset:5120\n" \
  "v_mfma_f32_32x32x16_bf16 v[16:31], v[140:143], v[72:75], v[16:31]\n" \
  "ds_read_b64_tr_b16 v[132:133], v145 offset:4096\n" \
  "ds_read_b64_tr_b16 v[134:135], v145 offset:5120\n" \
  "v_mfma_f32_32x32x16_bf16 v[32:47], v[136:139], v[76:79], v[32:47]\n" \
  "v_mfma_f32_32x32x16_bf16 v[48:63], v[140:143], v[76:79], v[48:63]\n" \
  "s_add_u32 m0, %[m0b], 2048\n" \
  "s_nop 0\n" \
  "global_load_lds_dwordx4 v148, s[40:41]\n" \
  "s_add_u32 m0, %[m0b], 3072\n" \
  "s_nop 0\n" \
  "global_load_lds_dwordx4 v149, s[40:41]\n" \
  "s_add_u32 s40, s40, 2048\n" \
  "s_addc_u32 s41, s41, 0\n" \
  "global_load_dwordx4 v[72:75], v146, s[42:43] sc1\n" \
  "global_load_dwordx4 v[76:79], v147, s[42:43] sc1\n" \
  "s_add_u32 s42, s42, 1024\n" \
  "s_addc_u32 s43, s43, 0\n" \
  "s_waitcnt vmcnt(16)\n" \
  "s_waitcnt lgkmcnt(0)\n" \
  "v_mfma_f32_32x32x16_bf16 v[0:15], v[128:131], v[80:83], v[0:15]\n" \
  "ds_read_b64_tr_b16 v[136:137], v144 offset:6144\n" \
  "ds_read_b64_tr_b16 v[138:139], v144 offset:7168\n" \
  "v_mfma_f32_32x32x16_bf16 v[16:31], v[132:135], v[80:83], v[16:31]\n" \
  "ds_read_b64_tr_b16 v[140:141], v145 offset:6144\n" \
  "ds_read_b64_tr_b16 v[142:143], v145 offset:7168\n" \
  "v_mfma_f32_32x32x16_bf16 v[32:47], v[128:131], v[84:87], v[32:47]\n" \
  "v_mfma_f32_32x32x16_bf16 v[48:63], v[132:135], v[84:87], v[48:63]\n" \
  "s_add_u32 m0, %[m0b], 4096\n" \
  "s_nop 0\n" \
  "global_load_lds_dwordx4 v148, s[40:41]\n" \
  "s_add_u32 m0, %[m0b], 5120\n" \
  "s_nop 0\n" \
  "global_load_lds_dwordx4 v149, s[40:41]\n" \
  "s_add_u32 s40, s40, 2048\n" \
  "s_addc_u32 s41, s41, 0\n" \
  "global_load_dwordx4 v[80:83], v146, s[42:43] sc1\n" \
  "global_load_dwordx4 v[84:87], v147, s[42:43] sc1\n" \
  "s_add_u32 s42, s42, 1024\n" \
  "s_addc_u32 s43, s43, 0\n" \
  "s_waitcnt vmcnt(18)\n" \
  "s_waitcnt lgkmcnt(0)\n" \
  "v_mfma_f32_32x32x16_bf16 v[0:15], v[136:139], v[88:91], v[0:15]\n" \
  "ds_read_b64_tr_b16 v[128:129], v144 offset:8192\n" \
  "ds_read_b64_tr_b16 v[130:131], v144 offset:9216\n" \
  "v_mfma_f32_32x32x16_bf16 v[16:31], v[140:143], v[88:91], v[16:31]\n" \
  "ds_read_b64_tr_b16 v[132:133], v145 offset:8192\n" \
  "ds_read_b64_tr_b16 v[134:135], v145 offset:9216\n" \
  "v_mfma_f32_32x32x16_bf16 v[32:47], v[136:139], v[92:95], v[32:47]\n" \
  "v_mfma_f32_32x32x16_bf16 v[48:63], v[140:143], v[92:95], v[48:63]\n" \
  "s_add_u32 m0, %[m0b], 6144\n" \
  "s_nop 0\n" \
  "global_load_lds_dwordx4 v148, s[40:41]\n" \
  "s_add_u32 m0, %[m0b], 7168\n" \
  "s_nop 0\n" \
  "global_load_lds_dwordx4 v149, s[40:41]\n" \
  "s_add_u32 s40, s40, 2048\n" \
  "s_addc_u32 s41, s41, 0\n" \
  "global_load_dwordx4 v[88:91], v146, s[42:43] sc1\n" \
  "global_load_dwordx4 v[92:95], v147, s[42:43] sc1\n" \
  "s_add_u32 s42, s42, 1024\n" \
  "s_addc_u32 s43, s43, 0\n" \
  "s_waitcnt vmcnt(20)\n" \
  "s_waitcnt lgkmcnt(0)\n" \
  "v_mfma_f32_32x32x16_bf16 v[0:15], v[128:131], v[96:99], v[0:15]\n" \
  "ds_read_b64_tr_b16 v[136:137], v144 offset:10240\n" \
  "ds_read_b64_tr_b16 v[138:139], v144 offset:11264\n" \
  "v_mfma_f32_32x32x16_bf16 v[16:31], v[132:135], v[96:99], v[16:31]\n" \
  "ds_read_b64_tr_b16 v[140:141], v145 offset:10240\n" \
  "ds_read_b64_tr_b16 v[142:143], v145 offset:11264\n" \
  "v_mfma_f32_32x32x16_bf16 v[32:47], v[128:131], v[100:103], v[32:47]\n" \
  "v_mfma_f32_32x32x16_bf16 v[48:63], v[132:135], v[100:103], v[48:63]\n" \
  "s_add_u32 m0, %[m0b], 8192\n" \
  "s_nop 0\n" \
  "global_load_lds_dwordx4 v148, s[40:41]\n" \
  "s_add_u32 m0, %[m0b], 9216\n" \
  "s_nop 0\n" \
  "global_load_lds_dwordx4 v149, s[40:41]\n" \
  "s_add_u32 s40, s40, 2048\n" \
  "s_addc_u32 s41, s41, 0\n" \
  "global_load_dwordx4 v[96:99], v146, s[42:43] sc1\n" \
  "global_load_dwordx4 v[100:103], v147, s[42:43] sc1\n" \
  "s_add_u32 s42, s42, 1024\n" \
  "s_addc_u32 s43, s43, 0\n" \
  "s_waitcnt vmcnt(22)\n" \
  "s_waitcnt lgkmcnt(0)\n" \
  "v_mfma_f32_32x32x16_bf16 v[0:15], v[136:139], v[104:107], v[0:15]\n" \
  "ds_read_b64_tr_b16 v[128:129], v144 offset:12288\n" \
  "ds_read_b64_tr_b16 v[130:131], v144 offset:13312\n" \
  "v_mfma_f32_32x32x16_bf16 v[16:31], v[140:143], v[104:107], v[16:31]\n" \
  "ds_read_b64_tr_b16 v[132:133], v145 offset:12288\n" \
  "ds_read_b64_tr_b16 v[134:135], v145 offset:13312\n" \
  "v_mfma_f32_32x32x16_bf16 v[32:47], v[136:139], v[108:111], v[32:47]\n" \
  "v_mfma_f32_32x32x16_bf16 v[48:63], v[140:143], v[108:111], v[48:63]\n" \
  "s_add_u32 m0, %[m0b], 10240\n" \
  "s_nop 0\n" \
  "global_load_lds_dwordx4 v148, s[40:41]\n" \
  "s_add_u32 m0, %[m0b], 11264\n" \
  "s_nop 0\n" \
  "global_load_lds_dwordx4 v149, s[40:41]\n" \
  "s_add_u32 s40, s40, 2048\n" \
  "s_addc_u32 s41, s41, 0\n" \
  "global_load_dwordx4 v[104:107], v146, s[42:43] sc1\n" \
  "global_load_dwordx4 v[108:111], v147, s[42:43] sc1\n" \
  "s_add_u32 s42, s42, 1024\n" \
  "s_addc_u32 s43, s43, 0\n" \
  "s_waitcnt vmcnt(24)\n" \
  "s_waitcnt lgkmcnt(0)\n" \
  "v_mfma_f32_32x32x16_bf16 v[0:15], v[128:131], v[112:115], v[0:15]\n" \
  "ds_read_b64_tr_b16 v[136:137], v144 offset:14336\n" \
  "ds_read_b64_tr_b16 v[138:139], v144 offset:15360\n" \
  "v_mfma_f32_32x32x16_bf16 v[16:31], v[132:135], v[112:115], v[16:31]\n" \
  "ds_read_b64_tr_b16 v[140:141], v145 offset:14336\n" \
  "ds_read_b64_tr_b16 v[142:143], v145 offset:15360\n" \
  "v_mfma_f32_32x32x16_bf16 v[32:47], v[128:131], v[116:119], v[32:47]\n" \
  "v_mfma_f32_32x32x16_bf16 v[48:63], v[132:135], v[116:119], v[48:63]\n" \
  "s_add_u32 m0, %[m0b], 12288\n" \
  "s_nop 0\n" \
  "global_load_lds_dwordx4 v148, s[40:41]\n" \
  "s_add_u32 m0, %[m0b], 13312\n" \
  "s_nop 0\n" \
  "global_load_lds_dwordx4 v149, s[40:41]\n" \
  "s_add_u32 s40, s40, 2048\n" \
  "s_addc_u32 s41, s41, 0\n" \
  "global_load_dwordx4 v[112:115], v146, s[42:43] sc1\n" \
  "global_load_dwordx4 v[116:119], v147, s[42:43] sc1\n" \
  "s_add_u32 s42, s42, 1024\n" \
  "s_addc_u32 s43, s43, 0\n" \
  "s_waitcnt vmcnt(24)\n" \
  "s_waitcnt lgkmcnt(0)\n" \
  "v_mfma_f32_32x32x16_bf16 v[0:15], v[136:139], v[120:123], v[0:15]\n" \
  "ds_read_b64_tr_b16 v[128:129], v144 offset:0\n" \
  "ds_read_b64_tr_b16 v[130:131], v144 offset:1024\n" \
  "v_mfma_f32_32x32x16_bf16 v[16:31], v[140:143], v[120:123], v[16:31]\n" \
  "ds_read_b64_tr_b16 v[132:133], v145 offset:0\n" \
  "ds_read_b64_tr_b16 v[134:135], v145 offset:1024\n" \
  "v_mfma_f32_32x32x16_bf16 v[32:47], v[136:139], v[124:127], v[32:47]\n" \
  "v_mfma_f32_32x32x16_bf16 v[48:63], v[140:143], v[124:127], v[48:63]\n" \
  "s_add_u32 m0, %[m0b], 14336\n" \
  "s_nop 0\n" \
  "global_load_lds_dwordx4 v148, s[40:41]\n" \
  "s_add_u32 m0, %[m0b], 15360\n" \
  "s_nop 0\n" \
  "global_load_lds_dwordx4 v149, s[40:41]\n" \
  "s_add_u32 s40, s40, 2048\n" \
  "s_addc_u32 s41, s41, 0\n" \
  "global_load_dwordx4 v[120:123], v146, s[42:43] sc1\n" \
  "global_load_dwordx4 v[124:127], v147, s[42:43] sc1\n" \
  "s_add_u32 s42, s42, 1024\n" \
  "s_addc_u32 s43, s43, 0\n" \
  "s_waitcnt vmcnt(24)\n" \
  "s_waitcnt lgkmcnt(0)\n" \
  "v_mfma_f32_32x32x16_bf16 v[0:15], v[128:131], v[64:67], v[0:15]\n" \
  "ds_read_b64_tr_b16 v[136:137], v144 offset:2048\n" \
  "ds_read_b64_tr_b16 v[138:139], v144 offset:3072\n" \
  "v_mfma_f32_32x32x16_bf16 v[16:31], v[132:135], v[64:67], v[16:31]\n" \
  "ds_read_b64_tr_b16 v[140:141], v145 offset:2048\n" \
  "ds_read_b64_tr_b16 v[142:143], v145 offset:3072\n" \
  "v_mfma_f32_32x32x16_bf16 v[32:47], v[128:131], v[68:71], v[32:47]\n" \
  "v_mfma_f32_32x32x16_bf16 v[48:63], v[132:135], v[68:71], v[48:63]\n" \
  "s_add_u32 m0, %[m0b], 0\n" \
  "s_nop 0\n" \
  "global_load_lds_dwordx4 v148, s[40:41]\n" \
  "s_add_u32 m0, %[m0b], 1024\n" \
  "s_nop 0\n" \
  "global_load_lds_dwordx4 v149, s[40:41]\n" \
  "s_add_u32 s40, s40, 2048\n" \
  "s_addc_u32 s41, s41, 0\n" \
  "global_load_dwordx4 v[64:67], v146, s[42:43] sc1\n" \
  "global_load_dwordx4 v[68:71], v147, s[42:43] sc1\n" \
  "s_add_u32 s42, s42, 1024\n" \
  "s_addc_u32 s43, s43, 0\n" \
  "s_waitcnt vmcnt(24)\n" \
  "s_waitcnt lgkmcnt(0)\n" \
  "v_mfma_f32_32x32x16_bf16 v[0:15], v[136:139], v[72:75], v[0:15]\n" \
  "ds_read_b64_tr_b16 v[128:129], v144 offset:4096\n" \
  "ds_read_b64_tr_b16 v[130:131], v144 offset:5120\n" \
  "v_mfma_f32_32x32x16_bf16 v[16:31], v[140:143], v[72:75], v[16:31]\n" \
  "ds_read_b64_tr_b16 v[132:133], v145 offset:4096\n" \
  "ds_read_b64_tr_b16 v[134:135], v145 offset:5120\n" \
  "v_mfma_f32_32x32x16_bf16 v[32:47], v[136:139], v[76:79], v[32:47]\n" \
  "v_mfma_f32_32x32x16_bf16 v[48:63], v[140:143], v[76:79], v[48:63]\n" \
  "s_add_u32 m0, %[m0b], 2048\n" \
  "s_nop 0\n" \
  "global_load_lds_dwordx4 v148, s[40:41]\n" \
  "s_add_u32 m0, %[m0b], 3072\n" \
  "s_nop 0\n" \
  "global_load_lds_dwordx4 v149, s[40:41]\n" \
  "s_add_u32 s40, s40, 2048\n" \
  "s_addc_u32 s41, s41, 0\n" \
  "global_load_dwordx4 v[72:75], v146, s[42:43] sc1\n" \
  "global_load_dwordx4 v[76:79], v147, s[42:43] sc1\n" \
  "s_add_u32 s42, s42, 1024\n" \
  "s_addc_u32 s43, s43, 0\n" \
  "s_waitcnt vmcnt(24)\n" \
  "s_waitcnt lgkmcnt(0)\n" \
  "v_mfma_f32_32x32x16_bf16 v[0:15], v[128:131], v[80:83], v[0:15]\n" \
  "ds_read_b64_tr_b16 v[136:137], v144 offset:6144\n" \
  "ds_read_b64_tr_b16 v[138:139], v144 offset:7168\n" \
  "v_mfma_f32_32x32x16_bf16 v[16:31], v[132:135], v[80:83], v[16:31]\n" \
  "ds_read_b64_tr_b16 v[140:141], v145 offset:6144\n" \
  "ds_read_b64_tr_b16 v[142:143], v145 offset:7168\n" \
  "v_mfma_f32_32x32x16_bf16 v[32:47], v[128:131], v[84:87], v[32:47]\n" \
  "v_mfma_f32_32x32x16_bf16 v[48:63], v[132:135], v[84:87], v[48:63]\n" \
  "s_add_u32 m0, %[m0b], 4096\n" \
  "s_nop 0\n" \
  "global_load_lds_dwordx4 v148, s[40:41]\n" \
  "s_add_u32 m0, %[m0b], 5120\n" \
  "s_nop 0\n" \
  "global_load_lds_dwordx4 v149, s[40:41]\n" \
  "s_add_u32 s40, s40, 2048\n" \
  "s_addc_u32 s41, s41, 0\n" \
  "global_load_dwordx4 v[80:83], v146, s[42:43] sc1\n" \
  "global_load_dwordx4 v[84:87], v147, s[42:43] sc1\n" \
  "s_add_u32 s42, s42, 1024\n" \
  "s_addc_u32 s43, s43, 0\n" \
  "s_waitcnt vmcnt(24)\n" \
  "s_waitcnt lgkmcnt(0)\n" \
  "v_mfma_f32_32x32x16_bf16 v[0:15], v[136:139], v[88:91], v[0:15]\n" \
  "ds_read_b64_tr_b16 v[128:129], v144 offset:8192\n" \
  "ds_read_b64_tr_b16 v[130:131], v144 offset:9216\n" \
  "v_mfma_f32_32x32x16_bf16 v[16:31], v[140:143], v[88:91], v[16:31]\n" \
  "ds_read_b64_tr_b16 v[132:133], v145 offset:8192\n" \
  "ds_read_b64_tr_b16 v[134:135], v145 offset:9216\n" \
  "v_mfma_f32_32x32x16_bf16 v[32:47], v[136:139], v[92:95], v[32:47]\n" \
  "v_mfma_f32_32x32x16_bf16 v[48:63], v[140:143], v[92:95], v[48:63]\n" \
  "s_add_u32 m0, %[m0b], 6144\n" \
  "s_nop 0\n" \
  "global_load_lds_dwordx4 v148, s[40:41]\n" \
  "s_add_u32 m0, %[m0b], 7168\n" \
  "s_nop 0\n" \
  "global_load_lds_dwordx4 v149, s[40:41]\n" \
  "s_add_u32 s40, s40, 2048\n" \
  "s_addc_u32 s41, s41, 0\n" \
  "global_load_dwordx4 v[88:91], v146, s[42:43] sc1\n" \
  "global_load_dwordx4 v[92:95], v147, s[42:43] sc1\n" \
  "s_add_u32 s42, s42, 1024\n" \
  "s_addc_u32 s43, s43, 0\n" \
  "s_waitcnt vmcnt(24)\n" \
  "s_waitcnt lgkmcnt(0)\n" \
  "v_mfma_f32_32x32x16_bf16 v[0:15], v[128:131], v[96:99], v[0:15]\n" \
  "ds_read_b64_tr_b16 v[136:137], v144 offset:10240\n" \
  "ds_read_b64_tr_b16 v[138:139], v144 offset:11264\n" \
  "v_mfma_f32_32x32x16_bf16 v[16:31], v[132:135], v[96:99], v[16:31]\n" \
  "ds_read_b64_tr_b16 v[140:141], v145 offset:10240\n" \
  "ds_read_b64_tr_b16 v[142:143], v145 offset:11264\n" \
  "v_mfma_f32_32x32x16_bf16 v[32:47], v[128:131], v[100:103], v[32:47]\n" \
  "v_mfma_f32_32x32x16_bf16 v[48:63], v[132:135], v[100:103], v[48:63]\n" \
  "s_add_u32 m0, %[m0b], 8192\n" \
  "s_nop 0\n" \
  "global_load_lds_dwordx4 v148, s[40:41]\n" \
  "s_add_u32 m0, %[m0b], 9216\n" \
  "s_nop 0\n" \
  "global_load_lds_dwordx4 v149, s[40:41]\n" \
  "s_add_u32 s40, s40, 2048\n" \
  "s_addc_u32 s41, s41, 0\n" \
  "global_load_dwordx4 v[96:99], v146, s[42:43] sc1\n" \
  "global_load_dwordx4 v[100:103], v147, s[42:43] sc1\n" \
  "s_add_u32 s42, s42, 1024\n" \
  "s_addc_u32 s43, s43, 0\n" \
  "s_waitcnt vmcnt(24)\n" \
  "s_waitcnt lgkmcnt(0)\n" \
  "v_mfma_f32_32x32x16_bf16 v[0:15], v[136:139], v[104:107], v[0:15]\n" \
  "ds_read_b64_tr_b16 v[128:129], v144 offset:12288\n" \
  "ds_read_b64_tr_b16 v[130:131], v144 offset:13312\n" \
  "v_mfma_f32_32x32x16_bf16 v[16:31], v[140:143], v[104:107], v[16:31]\n" \
  "ds_read_b64_tr_b16 v[132:133], v145 offset:12288\n" \
  "ds_read_b64_tr_b16 v[134:135], v145 offset:13312\n" \
  "v_mfma_f32_32x32x16_bf16 v[32:47], v[136:139], v[108:111], v[32:47]\n" \
  "v_mfma_f32_32x32x16_bf16 v[48:63], v[140:143], v[108:111], v[48:63]\n" \
  "s_add_u32 m0, %[m0b], 10240\n" \
  "s_nop 0\n" \
  "global_load_lds_dwordx4 v148, s[40:41]\n" \
  "s_add_u32 m0, %[m0b], 11264\n" \
  "s_nop 0\n" \
  "global_load_lds_dwordx4 v149, s[40:41]\n" \
  "s_add_u32 s40, s40, 2048\n" \
  "s_addc_u32 s41, s41, 0\n" \
  "global_load_dwordx4 v[104:107], v146, s[42:43] sc1\n" \
  "global_load_dwordx4 v[108:111], v147, s[42:43] sc1\n" \
  "s_add_u32 s42, s42, 1024\n" \
  "s_addc_u32 s43, s43, 0\n" \
  "s_waitcnt vmcnt(24)\n" \
  "s_waitcnt lgkmcnt(0)\n" \
  "v_mfma_f32_32x32x16_bf16 v[0:15], v[128:131], v[112:115], v[0:15]\n" \
  "ds_read_b64_tr_b16 v[136:137], v144 offset:14336\n" \
  "ds_read_b64_tr_b16 v[138:139], v144 offset:15360\n" \
  "v_mfma_f32_32x32x16_bf16 v[16:31], v[132:135], v[112:115], v[16:31]\n" \
  "ds_read_b64_tr_b16 v[140:141], v145 offset:14336\n" \
  "ds_read_b64_tr_b16 v[142:143], v145 offset:15360\n" \
  "v_mfma_f32_32x32x16_bf16 v[32:47], v[128:131], v[116:119], v[32:47]\n" \
  "v_mfma_f32_32x32x16_bf16 v[48:63], v[132:135], v[116:119], v[48:63]\n" \
  "s_add_u32 m0, %[m0b], 12288\n" \
  "s_nop 0\n" \
  "global_load_lds_dwordx4 v148, s[40:41]\n" \
  "s_add_u32 m0, %[m0b], 13312\n" \
  "s_nop 0\n" \
  "global_load_lds_dwordx4 v149, s[40:41]\n" \
  "s_add_u32 s40, s40, 2048\n" \
  "s_addc_u32 s41, s41, 0\n" \
  "global_load_dwordx4 v[112:115], v146, s[42:43] sc1\n" \
  "global_load_dwordx4 v[116:119], v147, s[42:43] sc1\n" \
  "s_add_u32 s42, s42, 1024\n" \
  "s_addc_u32 s43, s43, 0\n" \
  "s_waitcnt vmcnt(24)\n" \
  "s_waitcnt lgkmcnt(0)\n" \
  "v_mfma_f32_32x32x16_bf16 v[0:15], v[136:139], v[120:123], v[0:15]\n" \
  "ds_read_b64_tr_b16 v[128:129], v144 offset:0\n" \
  "ds_read_b64_tr_b16 v[130:131], v144 offset:1024\n" \
  "v_mfma_f32_32x32x16_bf16 v[16:31], v[140:143], v[120:123], v[16:31]\n" \
  "ds_read_b64_tr_b16 v[132:133], v145 offset:0\n" \
  "ds_read_b64_tr_b16 v[134:135], v145 offset:1024\n" \
  "v_mfma_f32_32x32x16_bf16 v[32:47], v[136:139], v[124:127], v[32:47]\n" \
  "v_mfma_f32_32x32x16_bf16 v[48:63], v[140:143], v[124:127], v[48:63]\n" \
  "s_add_u32 m0, %[m0b], 14336\n" \
  "s_nop 0\n" \
  "global_load_lds_dwordx4 v148, s[40:41]\n" \
  "s_add_u32 m0, %[m0b], 15360\n" \
  "s_nop 0\n" \
  "global_load_lds_dwordx4 v149, s[40:41]\n" \
  "s_add_u32 s40, s40, 2048\n" \
  "s_addc_u32 s41, s41, 0\n" \
  "global_load_dwordx4 v[120:123], v146, s[42:43] sc1\n" \
  "global_load_dwordx4 v[124:127], v147, s[42:43] sc1\n" \
  "s_add_u32 s42, s42, 1024\n" \
  "s_addc_u32 s43, s43, 0\n" \
  "s_waitcnt vmcnt(24)\n" \
  "s_waitcnt lgkmcnt(0)\n" \
  "v_mfma_f32_32x32x16_bf16 v[0:15], v[128:131], v[64:67], v[0:15]\n" \
  "ds_read_b64_tr_b16 v[136:137], v144 offset:2048\n" \
  "ds_read_b64_tr_b16 v[138:139], v144 offset:3072\n" \
  "v_mfma_f32_32x32x16_bf16 v[16:31], v[132:135], v[64:67], v[16:31]\n" \
  "ds_read_b64_tr_b16 v[140:141], v145 offset:2048\n" \
  "ds_read_b64_tr_b16 v[142:143], v145 offset:3072\n" \
  "v_mfma_f32_32x32x16_bf16 v[32:47], v[128:131], v[68:71], v[32:47]\n" \
  "v_mfma_f32_32x32x16_bf16 v[48:63], v[132:135], v[68:71], v[48:63]\n" \
  "s_add_u32 m0, %[m0b], 0\n" \
  "s_nop 0\n" \
  "global_load_lds_dwordx4 v148, s[40:41]\n" \
  "s_add_u32 m0, %[m0b], 1024\n" \
  "s_nop 0\n" \
  "global_load_lds_dwordx4 v149, s[40:41]\n" \
  "s_add_u32 s40, s40, 2048\n" \
  "s_addc_u32 s41, s41, 0\n" \
  "global_load_dwordx4 v[64:67], v146, s[42:43] sc1\n" \
  "global_load_dwordx4 v[68:71], v147, s[42:43] sc1\n" \
  "s_add_u32 s42, s42, 1024\n" \
  "s_addc_u32 s43, s43, 0\n" \
  "s_waitcnt vmcnt(24)\n" \
  "s_waitcnt lgkmcnt(0)\n" \
  "v_mfma_f32_32x32x16_bf16 v[0:15], v[136:139], v[72:75], v[0:15]\n" \
  "ds_read_b64_tr_b16 v[128:129], v144 offset:4096\n" \
  "ds_read_b64_tr_b16 v[130:131], v144 offset:5120\n" \
  "v_mfma_f32_32x32x16_bf16 v[16:31], v[140:143], v[72:75], v[16:31]\n" \
  "ds_read_b64_tr_b16 v[132:133], v145 offset:4096\n" \
  "ds_read_b64_tr_b16 v[134:135], v145 offset:5120\n" \
  "v_mfma_f32_32x32x16_bf16 v[32:47], v[136:139], v[76:79], v[32:47]\n" \
  "v_mfma_f32_32x32x16_bf16 v[48:63], v[140:143], v[76:79], v[48:63]\n" \
  "s_add_u32 m0, %[m0b], 2048\n" \
  "s_nop 0\n" \
  "global_load_lds_dwordx4 v148, s[40:41]\n" \
  "s_add_u32 m0, %[m0b], 3072\n" \
  "s_nop 0\n" \
  "global_load_lds_dwordx4 v149, s[40:41]\n" \
  "s_add_u32 s40, s40, 2048\n" \
  "s_addc_u32 s41, s41, 0\n" \
  "global_load_dwordx4 v[72:75], v146, s[42:43] sc1\n" \
  "global_load_dwordx4 v[76:79], v147, s[42:43] sc1\n" \
  "s_add_u32 s42, s42, 1024\n" \
  "s_addc_u32 s43, s43, 0\n" \
  "s_waitcnt vmcnt(24)\n" \
  "s_waitcnt lgkmcnt(0)\n" \
  "v_mfma_f32_32x32x16_bf16 v[0:15], v[128:131], v[80:83], v[0:15]\n" \
  "ds_read_b64_tr_b16 v[136:137], v144 offset:6144\n" \
  "ds_read_b64_tr_b16 v[138:139], v144 offset:7168\n" \
  "v_mfma_f32_32x32x16_bf16 v[16:31], v[132:135], v[80:83], v[16:31]\n" \
  "ds_read_b64_tr_b16 v[140:141], v145 offset:6144\n" \
  "ds_read_b64_tr_b16 v[142:143], v145 offset:7168\n" \
  "v_mfma_f32_32x32x16_bf16 v[32:47], v[128:131], v[84:87], v[32:47]\n" \
  "v_mfma_f32_32x32x16_bf16 v[48:63], v[132:135], v[84:87], v[48:63]\n" \
  "s_add_u32 m0, %[m0b], 4096\n" \
  "s_nop 0\n" \
  "global_load_lds_dwordx4 v148, s[40:41]\n" \
  "s_add_u32 m0, %[m0b], 5120\n" \
  "s_nop 0\n" \
  "global_load_lds_dwordx4 v149, s[40:41]\n" \
  "s_add_u32 s40, s40, 2048\n" \
  "s_addc_u32 s41, s41, 0\n" \
  "global_load_dwordx4 v[80:83], v146, s[42:43] sc1\n" \
  "global_load_dwordx4 v[84:87], v147, s[42:43] sc1\n" \
  "s_add_u32 s42, s42, 1024\n" \
  "s_addc_u32 s43, s43, 0\n" \
  "s_waitcnt vmcnt(24)\n" \
  "s_waitcnt lgkmcnt(0)\n" \
  "v_mfma_f32_32x32x16_bf16 v[0:15], v[136:139], v[88:91], v[0:15]\n" \
  "ds_read_b64_tr_b16 v[128:129], v144 offset:8192\n" \
  "ds_read_b64_tr_b16 v[130:131], v144 offset:9216\n" \
  "v_mfma_f32_32x32x16_bf16 v[16:31], v[140:143], v[88:91], v[16:31]\n" \
  "ds_read_b64_tr_b16 v[132:133], v145 offset:8192\n" \
  "ds_read_b64_tr_b16 v[134:135], v145 offset:9216\n" \
  "v_mfma_f32_32x32x16_bf16 v[32:47], v[136:139], v[92:95], v[32:47]\n" \
  "v_mfma_f32_32x32x16_bf16 v[48:63], v[140:143], v[92:95], v[48:63]\n" \
  "s_add_u32 m0, %[m0b], 6144\n" \
  "s_nop 0\n" \
  "global_load_lds_dwordx4 v148, s[40:41]\n" \
  "s_add_u32 m0, %[m0b], 7168\n" \
  "s_nop 0\n" \
  "global_load_lds_dwordx4 v149, s[40:41]\n" \
  "s_add_u32 s40, s40, 2048\n" \
  "s_addc_u32 s41, s41, 0\n" \
  "global_load_dwordx4 v[88:91], v146, s[42:43] sc1\n" \
  "global_load_dwordx4 v[92:95], v147, s[42:43] sc1\n" \
  "s_add_u32 s42, s42, 1024\n" \
  "s_addc_u32 s43, s43, 0\n" \
  "s_waitcnt vmcnt(24)\n" \
  "s_waitcnt lgkmcnt(0)\n" \
  "v_mfma_f32_32x32x16_bf16 v[0:15], v[128:131], v[96:99], v[0:15]\n" \
  "ds_read_b64_tr_b16 v[136:137], v144 offset:10240\n" \
  "ds_read_b64_tr_b16 v[138:139], v144 offset:11264\n" \
  "v_mfma_f32_32x32x16_bf16 v[16:31], v[132:135], v[96:99], v[16:31]\n" \
  "ds_read_b64_tr_b16 v[140:141], v145 offset:10240\n" \
  "ds_read_b64_tr_b16 v[142:143], v145 offset:11264\n" \
  "v_mfma_f32_32x32x16_bf16 v[32:47], v[128:131], v[100:103], v[32:47]\n" \
  "v_mfma_f32_32x32x16_bf16 v[48:63], v[132:135], v[100:103], v[48:63]\n" \
  "s_waitcnt vmcnt(20)\n" \
  "s_waitcnt lgkmcnt(0)\n" \
  "v_mfma_f32_32x32x16_bf16 v[0:15], v[136:139], v[104:107], v[0:15]\n" \
  "ds_read_b64_tr_b16 v[128:129], v144 offset:12288\n" \
  "ds_read_b64_tr_b16 v[130:131], v144 offset:13312\n" \
  "v_mfma_f32_32x32x16_bf16 v[16:31], v[140:143], v[104:107], v[16:31]\n" \
  "ds_read_b64_tr_b16 v[132:133], v145 offset:12288\n" \
  "ds_read_b64_tr_b16 v[134:135], v145 offset:13312\n" \
  "v_mfma_f32_32x32x16_bf16 v[32:47], v[136:139], v[108:111], v[32:47]\n" \
  "v_mfma_f32_32x32x16_bf16 v[48:63], v[140:143], v[108:111], v[48:63]\n" \
  "s_waitcnt vmcnt(16)\n" \
  "s_waitcnt lgkmcnt(0)\n" \
  "v_mfma_f32_32x32x16_bf16 v[0:15], v[128:131], v[112:115], v[0:15]\n" \
  "ds_read_b64_tr_b16 v[136:137], v144 offset:14336\n" \
  "ds_read_b64_tr_b16 v[138:139], v144 offset:15360\n" \
  "v_mfma_f32_32x32x16_bf16 v[16:31], v[132:135], v[112:115], v[16:31]\n" \
  "ds_read_b64_tr_b16 v[140:141], v145 offset:14336\n" \
  "ds_read_b64_tr_b16 v[142:143], v145 offset:15360\n" \
  "v_mfma_f32_32x32x16_bf16 v[32:47], v[128:131], v[116:119], v[32:47]\n" \
  "v_mfma_f32_32x32x16_bf16 v[48:63], v[132:135], v[116:119], v[48:63]\n" \
  "s_waitcnt vmcnt(12)\n" \
  "s_waitcnt lgkmcnt(0)\n" \
  "v_mfma_f32_32x32x16_bf16 v[0:15], v[136:139], v[120:123], v[0:15]\n" \
  "ds_read_b64_tr_b16 v[128:129], v144 offset:0\n" \
  "ds_read_b64_tr_b16 v[130:131], v144 offset:1024\n" \
  "v_mfma_f32_32x32x16_bf16 v[16:31], v[140:143], v[120:123], v[16:31]\n" \
  "ds_read_b64_tr_b16 v[132:133], v145 offset:0\n" \
  "ds_read_b64_tr_b16 v[134:135], v145 offset:1024\n" \
  "v_mfma_f32_32x32x16_bf16 v[32:47], v[136:139], v[124:127], v[32:47]\n" \
  "v_mfma_f32_32x32x16_bf16 v[48:63], v[140:143], v[124:127], v[48:63]\n" \
  "s_waitcnt vmcnt(8)\n" \
  "s_waitcnt lgkmcnt(0)\n" \
  "v_mfma_f32_32x32x16_bf16 v[0:15], v[128:131], v[64:67], v[0:15]\n" \
  "ds_read_b64_tr_b16 v[136:137], v144 offset:2048\n" \
  "ds_read_b64_tr_b16 v[138:139], v144 offset:3072\n" \
  "v_mfma_f32_32x32x16_bf16 v[16:31], v[132:135], v[64:67], v[16:31]\n" \
  "ds_read_b64_tr_b16 v[140:141], v145 offset:2048\n" \
  "ds_read_b64_tr_b16 v[142:143], v145 offset:3072\n" \
  "v_mfma_f32_32x32x16_bf16 v[32:47], v[128:131], v[68:71], v[32:47]\n" \
  "v_mfma_f32_32x32x16_bf16 v[48:63], v[132:135], v[68:71], v[48:63]\n" \
  "s_waitcnt vmcnt(4)\n" \
  "s_waitcnt lgkmcnt(0)\n" \
  "v_mfma_f32_32x32x16_bf16 v[0:15], v[136:139], v[72:75], v[0:15]\n" \
  "ds_read_b64_tr_b16 v[128:129], v144 offset:4096\n" \
  "ds_read_b64_tr_b16 v[130:131], v144 offset:5120\n" \
  "v_mfma_f32_32x32x16_bf16 v[16:31], v[140:143], v[72:75], v[16:31]\n" \
  "ds_read_b64_tr_b16 v[132:133], v145 offset:4096\n" \
  "ds_read_b64_tr_b16 v[134:135], v145 offset:5120\n" \
  "v_mfma_f32_32x32x16_bf16 v[32:47], v[136:139], v[76:79], v[32:47]\n" \
  "v_mfma_f32_32x32x16_bf16 v[48:63], v[140:143], v[76:79], v[48:63]\n" \
  "s_waitcnt vmcnt(0)\n" \
  "s_waitcnt lgkmcnt(0)\n" \
  "v_mfma_f32_32x32x16_bf16 v[0:15], v[128:131], v[80:83], v[0:15]\n" \
  "ds_read_b64_tr_b16 v[136:137], v144 offset:6144\n" \
  "ds_read_b64_tr_b16 v[138:139], v144 offset:7168\n" \
  "v_mfma_f32_32x32x16_bf16 v[16:31], v[132:135], v[80:83], v[16:31]\n" \
  "ds_read_b64_tr_b16 v[140:141], v145 offset:6144\n" \
  "ds_read_b64_tr_b16 v[142:143], v145 offset:7168\n" \
  "v_mfma_f32_32x32x16_bf16 v[32:47], v[128:131], v[84:87], v[32:47]\n" \
  "v_mfma_f32_32x32x16_bf16 v[48:63], v[132:135], v[84:87], v[48:63]\n" \
  "s_waitcnt lgkmcnt(0)\n" \
  "v_mfma_f32_32x32x16_bf16 v[0:15], v[136:139], v[88:91], v[0:15]\n" \
  "v_mfma_f32_32x32x16_bf16 v[16:31], v[140:143], v[88:91], v[16:31]\n" \
  "v_mfma_f32_32x32x16_bf16 v[32:47], v[136:139], v[92:95], v[32:47]\n" \
  "v_mfma_f32_32x32x16_bf16 v[48:63], v[140:143], v[92:95], v[48:63]\n" \
  "s_nop 15\n" \
  ""
#define G64_WO_ASM \
  "1:\n" \
  "s_mov_b64 s[42:43], %[ab0]\n" \
  "global_load_dwordx4 v[64:67], v146, s[42:43] offset:0 sc1\n" \
  "global_load_dwordx4 v[68:71], v146, s[42:43] offset:1024 sc1\n" \
  "global_load_dwordx4 v[72:75], v146, s[42:43] offset:2048 sc1\n" \
  "global_load_dwordx4 v[76:79], v146, s[42:43] offset:3072 sc1\n" \
  "s_add_u32 s42, s42, 4096\n" \
  "s_addc_u32 s43, s43, 0\n" \
  "global_load_dwordx4 v[80:83], v146, s[42:43] offset:0 sc1\n" \
  "global_load_dwordx4 v[84:87], v146, s[42:43] offset:1024 sc1\n" \
  "global_load_dwordx4 v[88:91], v146, s[42:43] offset:2048 sc1\n" \
  "global_load_dwordx4 v[92:95], v146, s[42:43] offset:3072 sc1\n" \
  "s_add_u32 s42, s42, 4096\n" \
  "s_addc_u32 s43, s43, 0\n" \
  "global_load_dwordx4 v[96:99], v146, s[42:43] offset:0 sc1\n" \
  "global_load_dwordx4 v[100:103], v146, s[42:43] offset:1024 sc1\n" \
  "global_load_dwordx4 v[104:107], v146, s[42:43] offset:2048 sc1\n" \
  "global_load_dwordx4 v[108:111], v146, s[42:43] offset:3072 sc1\n" \
  "s_add_u32 s42, s42, 4096\n" \
  "s_addc_u32 s43, s43, 0\n" \
  "global_load_dwordx4 v[112:115], v146, s[42:43] offset:0 sc1\n" \
  "global_load_dwordx4 v[116:119], v146, s[42:43] offset:1024 sc1\n" \
  "global_load_dwordx4 v[120:123], v146, s[42:43] offset:2048 sc1\n" \
  "global_load_dwordx4 v[124:127], v146, s[42:43] offset:3072 sc1\n" \
  "ds_read_b64_tr_b16 v[128:129], v144 offset:0\n" \
  "ds_read_b64_tr_b16 v[130:131], v144 offset:1024\n" \
  "ds_read_b64_tr_b16 v[132:133], v145 offset:0\n" \
  "ds_read_b64_tr_b16 v[134:135], v145 offset:1024\n" \
  "ds_read_b64_tr_b16 v[136:137], v144 offset:2048\n" \
  "ds_read_b64_tr_b16 v[138:139], v144 offset:3072\n" \
  "ds_read_b64_tr_b16 v[140:141], v145 offset:2048\n" \
  "ds_read_b64_tr_b16 v[142:143], v145 offset:3072\n" \
  "s_waitcnt vmcnt(15) lgkmcnt(4)\n" \
  "v_mfma_f32_32x32x16_bf16 v[0:15], v[128:131], v[64:67], 0\n" \
  "ds_read_b64_tr_b16 v[32:33], v144 offset:4096\n" \
  "ds_read_b64_tr_b16 v[34:35], v144 offset:5120\n" \
  "v_mfma_f32_32x32x16_bf16 v[16:31], v[132:135], v[64:67], 0\n" \
  "ds_read_b64_tr_b16 v[36:37], v145 offset:4096\n" \
  "ds_read_b64_tr_b16 v[38:39], v145 offset:5120\n" \
  "s_waitcnt vmcnt(14) lgkmcnt(4)\n" \
  "v_mfma_f32_32x32x16_bf16 v[0:15], v[136:139], v[68:71], v[0:15]\n" \
  "ds_read_b64_tr_b16 v[40:41], v144 offset:6144\n" \
  "ds_read_b64_tr_b16 v[42:43], v144 offset:7168\n" \
  "v_mfma_f32_32x32x16_bf16 v[16:31], v[140:143], v[68:71], v[16:31]\n" \
  "ds_read_b64_tr_b16 v[44:45], v145 offset:6144\n" \
  "ds_read_b64_tr_b16 v[46:47], v145 offset:7168\n" \
  "s_waitcnt vmcnt(13) lgkmcnt(4)\n" \
  "v_mfma_f32_32x32x16_bf16 v[0:15], v[32:35], v[72:75], v[0:15]\n" \
  "ds_read_b64_tr_b16 v[128:129], v144 offset:8192\n" \
  "ds_read_b64_tr_b16 v[130:131], v144 offset:9216\n" \
  "v_mfma_f32_32x32x16_bf16 v[16:31], v[36:39], v[72:75], v[16:31]\n" \
  "ds_read_b64_tr_b16 v[132:133], v145 offset:8192\n" \
  "ds_read_b64_tr_b16 v[134:135], v145 offset:9216\n" \
  "s_waitcnt vmcnt(12) lgkmcnt(4)\n" \
  "v_mfma_f32_32x32x16_bf16 v[0:15], v[40:43], v[76:79], v[0:15]\n" \
  "ds_read_b64_tr_b16 v[136:137], v144 offset:10240\n" \
  "ds_read_b64_tr_b16 v[138:139], v144 offset:11264\n" \
  "v_mfma_f32_32x32x16_bf16 v[16:31], v[44:47], v[76:79], v[16:31]\n" \
  "ds_read_b64_tr_b16 v[140:141], v145 offset:10240\n" \
  "ds_read_b64_tr_b16 v[142:143], v145 offset:11264\n" \
  "s_waitcnt vmcnt(11) lgkmcnt(4)\n" \
  "v_mfma_f32_32x32x16_bf16 v[0:15], v[128:131], v[80:83], v[0:15]\n" \
  "ds_read_b64_tr_b16 v[32:33], v144 offset:12288\n" \
  "ds_read_b64_tr_b16 v[34:35], v144 offset:13312\n" \
  "v_mfma_f32_32x32x16_bf16 v[16:31], v[132:135], v[80:83], v[16:31]\n" \
  "ds_read_b64_tr_b16 v[36:37], v145 offset:12288\n" \
  "ds_read_b64_tr_b16 v[38:39], v145 offset:13312\n" \
  "s_waitcnt vmcnt(10) lgkmcnt(4)\n" \
  "v_mfma_f32_32x32x16_bf16 v[0:15], v[136:139], v[84:87], v[0:15]\n" \
  "ds_read_b64_tr_b16 v[40:41], v144 offset:14336\n" \
  "ds_read_b64_tr_b16 v[42:43], v144 offset:15360\n" \
  "v_mfma_f32_32x32x16_bf16 v[16:31], v[140:143], v[84:87], v[16:31]\n" \
  "ds_read_b64_tr_b16 v[44:45], v145 offset:14336\n" \
  "ds_read_b64_tr_b16 v[46:47], v145 offset:15360\n" \
  "s_waitcnt vmcnt(9) lgkmcnt(4)\n" \
  "v_mfma_f32_32x32x16_bf16 v[0:15], v[32:35], v[88:91], v[0:15]\n" \
  "ds_read_b64_tr_b16 v[128:129], v144 offset:16384\n" \
  "ds_read_b64_tr_b16 v[130:131], v144 offset:17408\n" \
  "v_mfma_f32_32x32x16_bf16 v[16:31], v[36:39], v[88:91], v[16:31]\n" \
  "ds_read_b64_tr_b16 v[132:133], v145 offset:16384\n" \
  "ds_read_b64_tr_b16 v[134:135], v145 offset:17408\n" \
  "s_waitcnt vmcnt(8) lgkmcnt(4)\n" \
  "v_mfma_f32_32x32x16_bf16 v[0:15], v[40:43], v[92:95], v[0:15]\n" \
  "ds_read_b64_tr_b16 v[136:137], v144 offset:18432\n" \
  "ds_read_b64_tr_b16 v[138:139], v144 offset:19456\n" \
  "v_mfma_f32_32x32x16_bf16 v[16:31], v[44:47], v[92:95], v[16:31]\n" \
  "ds_read_b64_tr_b16 v[140:141], v145 offset:18432\n" \
  "ds_read_b64_tr_b16 v[142:143], v145 offset:19456\n" \
  "s_waitcnt vmcnt(7) lgkmcnt(4)\n" \
  "v_mfma_f32_32x32x16_bf16 v[0:15], v[128:131], v[96:99], v[0:15]\n" \
  "ds_read_b64_tr_b16 v[32:33], v144 offset:20480\n" \
  "ds_read_b64_tr_b16 v[34:35], v144 offset:21504\n" \
  "v_mfma_f32_32x32x16_bf16 v[16:31], v[132:135], v[96:99], v[16:31]\n" \
  "ds_read_b64_tr_b16 v[36:37], v145 offset:20480\n" \
  "ds_read_b64_tr_b16 v[38:39], v145 offset:21504\n" \
  "s_waitcnt vmcnt(6) lgkmcnt(4)\n" \
  "v_mfma_f32_32x32x16_bf16 v[0:15], v[136:139], v[100:103], v[0:15]\n" \
  "ds_read_b64_tr_b16 v[40:41], v144 offset:22528\n" \
  "ds_read_b64_tr_b16 v[42:43], v144 offset:23552\n" \
  "v_mfma_f32_32x32x16_bf16 v[16:31], v[140:143], v[100:103], v[16:31]\n" \
  "ds_read_b64_tr_b16 v[44:45], v145 offset:22528\n" \
  "ds_read_b64_tr_b16 v[46:47], v145 offset:23552\n" \
  "s_waitcnt vmcnt(5) lgkmcnt(4)\n" \
  "v_mfma_f32_32x32x16_bf16 v[0:15], v[32:35], v[104:107], v[0:15]\n" \
  "ds_read_b64_tr_b16 v[128:129], v144 offset:24576\n" \
  "ds_read_b64_tr_b16 v[130:131], v144 offset:25600\n" \
  "v_mfma_f32_32x32x16_bf16 v[16:31], v[36:39], v[104:107], v[16:31]\n" \
  "ds_read_b64_tr_b16 v[132:133], v145 offset:24576\n" \
  "ds_read_b64_tr_b16 v[134:135], v145 offset:25600\n" \
  "s_waitcnt vmcnt(4) lgkmcnt(4)\n" \
  "v_mfma_f32_32x32x16_bf16 v[0:15], v[40:43], v[108:111], v[0:15]\n" \
  "ds_read_b64_tr_b16 v[136:137], v144 offset:26624\n" \
  "ds_read_b64_tr_b16 v[138:139], v144 offset:27648\n" \
  "v_mfma_f32_32x32x16_bf16 v[16:31], v[44:47], v[108:111], v[16:31]\n" \
  "ds_read_b64_tr_b16 v[140:141], v145 offset:26624\n" \
  "ds_read_b64_tr_b16 v[142:143], v145 offset:27648\n" \
  "s_waitcnt vmcnt(3) lgkmcnt(4)\n" \
  "v_mfma_f32_32x32x16_bf16 v[0:15], v[128:131], v[112:115], v[0:15]\n" \
  "ds_read_b64_tr_b16 v[32:33], v144 offset:28672\n" \
  "ds_read_b64_tr_b16 v[34:35], v144 offset:29696\n" \
  "v_mfma_f32_32x32x16_bf16 v[16:31], v[132:135], v[112:115], v[16:31]\n" \
  "ds_read_b64_tr_b16 v[36:37], v145 offset:28672\n" \
  "ds_read_b64_tr_b16 v[38:39], v145 offset:29696\n" \
  "s_waitcnt vmcnt(2) lgkmcnt(4)\n" \
  "v_mfma_f32_32x32x16_bf16 v[0:15], v[136:139], v[116:119], v[0:15]\n" \
  "ds_read_b64_tr_b16 v[40:41], v144 offset:30720\n" \
  "ds_read_b64_tr_b16 v[42:43], v144 offset:31744\n" \
  "v_mfma_f32_32x32x16_bf16 v[16:31], v[140:143], v[116:119], v[16:31]\n" \
  "ds_read_b64_tr_b16 v[44:45], v145 offset:30720\n" \
  "ds_read_b64_tr_b16 v[46:47], v145 offset:31744\n" \
  "s_waitcnt vmcnt(1) lgkmcnt(4)\n" \
  "v_mfma_f32_32x32x16_bf16 v[0:15], v[32:35], v[120:123], v[0:15]\n" \
  "v_mfma_f32_32x32x16_bf16 v[16:31], v[36:39], v[120:123], v[16:31]\n" \
  "s_waitcnt vmcnt(0) lgkmcnt(0)\n" \
  "v_mfma_f32_32x32x16_bf16 v[0:15], v[40:43], v[124:127], v[0:15]\n" \
  "v_mfma_f32_32x32x16_bf16 v[16:31], v[44:47], v[124:127], v[16:31]\n" \
  "s_nop 15\n" \
  "ds_write_b128 v147, v[16:19] offset:0\n" \
  "ds_write_b128 v147, v[20:23] offset:1024\n" \
  "ds_write_b128 v147, v[24:27] offset:2048\n" \
  "ds_write_b128 v147, v[28:31] offset:3072\n" \
  "s_waitcnt lgkmcnt(0)\n" \
  "s_barrier\n" \
  "ds_read_b128 v[16:19], v148 offset:0\n" \
  "ds_read_b128 v[20:23], v148 offset:1024\n" \
  "ds_read_b128 v[24:27], v148 offset:2048\n" \
  "ds_read_b128 v[28:31], v148 offset:3072\n" \
  "s_waitcnt lgkmcnt(0)\n" \
  ""
constexpr int K2_LDS = 81920;

#define CLOB_V(a0, a1, a2, a3, a4, a5, a6, a7) "v" #a0, "v" #a1, "v" #a2, "v" #a3, "v" #a4, "v" #a5, "v" #a6, "v" #a7

struct G64 { unsigned tb0, tb1, dv0, dv1, m0b; };
DI G64 g64_setup(unsigned lds0, int w, int lane) {
  G64 g;
  const unsigned h = lane >> 5, tr_r = (lane & 15) >> 2, tr_c = lane & 3, sw = (tr_r >> 1) & 1;
  const unsigned base = lds0 + w * 16384 + (4 * h + tr_r) * 128 + ((lane >> 4) & 1) * 32 + (tr_c & 1) * 16 + (tr_c >> 1) * 8;
  g.tb0 = base + ((sw ^ (w >> 1)) << 6);
  g.tb1 = base + ((sw ^ (w >> 1) ^ 1) << 6);
  g.dv0 = lane * 16;
  g.dv1 = g.dv0 + 1024;
  g.m0b = lds0 + w * 16384;
  return g;
}
DI void g64_pre(const G64& g, const char* wb) {
  asm volatile(G64_PRE_ASM :: [wb0] "s"(wb), [m0b] "s"(g.m0b), "{v148}"(g.dv0), "{v149}"(g.dv1) : "memory", "scc", "s40", "s41");
}
#define G64_MAIN(ASMTEXT, acc, g, wb, ab, ao0, ao1) \
  asm volatile(ASMTEXT \
               : "={v[0:15]}"(acc[0]), "={v[16:31]}"(acc[1]), "={v[32:47]}"(acc[2]), "={v[48:63]}"(acc[3]) \
               : [wb0] "s"(wb), [ab0] "s"(ab), [m0b] "s"(g.m0b), "{v144}"(g.tb0), "{v145}"(g.tb1), "{v146}"(ao0), "{v147}"(ao1), \
                 "{v148}"(g.dv0), "{v149}"(g.dv1) \
               : "memory", "scc", "s40", "s41", "s42", "s43", \
                 CLOB_V(64, 65, 66, 67, 68, 69, 70, 71), CLOB_V(72, 73, 74, 75, 76, 77, 78, 79), CLOB_V(80, 81, 82, 83, 84, 85, 86, 87), \
                 CLOB_V(88, 89, 90, 91, 92, 93, 94, 95), CLOB_V(96, 97, 98, 99, 100, 101, 102, 103), CLOB_V(104, 105, 106, 107, 108, 109, 110, 111), \
                 CLOB_V(112, 113, 114, 115, 116, 117, 118, 119), CLOB_V(120, 121, 122, 123, 124, 125, 126, 127), \
                 CLOB_V(128, 129, 130, 131, 132, 133, 134, 135), CLOB_V(136, 137, 138, 139, 140, 141, 142, 143))

DI void g64_reduce(const f32x16 (&acc)[4], char* smem, int w, int lane, float (&y)[16]) {
  LDS(char)* ring = (LDS(char)*)smem + lane * 16;
#pragma unroll
  for (int i = 1; i < 4; ++i)
#pragma unroll
    for (int q = 0; q < 4; ++q)
      *(LDS(f32x4)*)(ring + w * 16384 + (i - 1) * 4096 + q * 1024) =
          f32x4{acc[i][4 * q], acc[i][4 * q + 1], acc[i][4 * q + 2], acc[i][4 * q + 3]};
  asm volatile("s_waitcnt lgkmcnt(0)\n\ts_barrier" ::: "memory");
#pragma unroll
  for (int q = 0; q < 4; ++q) {
    const f32x4 v1 = *(LDS(f32x4)*)(ring + (w ^ 2) * 16384 + q * 1024);
    const f32x4 v2 = *(LDS(f32x4)*)(ring + (w ^ 1) * 16384 + 4096 + q * 1024);
    const f32x4 v3 = *(LDS(f32x4)*)(ring + (w ^ 3) * 16384 + 8192 + q * 1024);
#pragma unroll
    for (int e = 0; e < 4; ++e) y[4 * q + e] = (acc[0][4 * q + e] + v1[e]) + (v2[e] + v3[e]);
  }
}

DI void ffn_role(int g, const char* __restrict__ wg, const char* __restrict__ wu, const char* __restrict__ wd,
                 const char* __restrict__ x, const char* __restrict__ wo, char* __restrict__ out,
                 char* __restrict__ wsp, char* smem) {
  const unsigned lds0 = (unsigned)(size_t)(LDS(char)*)smem;
  const int tid = threadIdx.x, lane = tid & 63, w = rfl(tid >> 6);
  const int xcd = g & 7, idx = g >> 3;
  const int mt = (xcd & 1) * 8 + (idx & 7), ft = (xcd >> 1) * 4 + (idx >> 3);
  const int mblock = mt * 4 + w;

  __builtin_amdgcn_s_setprio(0);
  __builtin_amdgcn_s_sleep(10);
  f32x16 acc[7];
  {
    const unsigned h = lane >> 5, tr_r = (lane & 15) >> 2, tr_c = lane & 3;
    const unsigned trb = lds0 + ((lane >> 4) & 1) * 7296 + (8 * h + tr_r) * 224 + tr_c * 8;
    const unsigned xoff = mblock * 32768 + lane * 16;
    const unsigned dl = lane < 56 ? lane : 0, drow = dl / 14, dc = dl - drow * 14;
    const unsigned dv = drow * 3584 + dc * 16;
    const char* wsrc = ((w >> 1) ? wu : wg) + ft * 224 + (w & 1) * 16 * 3584;
    const unsigned m0b = lds0 + (w >> 1) * 7296 + (w & 1) * 3584;
    asm volatile(G1_ASM
                 : "={v[0:15]}"(acc[0]), "={v[16:31]}"(acc[1]), "={v[32:47]}"(acc[2]), "={v[48:63]}"(acc[3]),
                   "={v[64:79]}"(acc[4]), "={v[80:95]}"(acc[5]), "={v[96:111]}"(acc[6])
                 : [wb0] "s"(wsrc), [xb0] "s"(wsp + WS_XN), [m0b] "s"(m0b), [dmask] "s"(0x00ffffffffffffffull),
                   "{v200}"(trb), "{v201}"(xoff), "{v202}"(dv), "{v203}"(dv + 4 * 3584), "{v204}"(dv + 8 * 3584), "{v205}"(dv + 12 * 3584)
                 : "memory", "scc", "s40", "s41", "s42", "s43",
                   CLOB_V(112, 113, 114, 115, 116, 117, 118, 119), CLOB_V(120, 121, 122, 123, 124, 125, 126, 127),
                   CLOB_V(128, 129, 130, 131, 132, 133, 134, 135), CLOB_V(136, 137, 138, 139, 140, 141, 142, 143),
                   CLOB_V(144, 145, 146, 147, 148, 149, 150, 151), CLOB_V(152, 153, 154, 155, 156, 157, 158, 159),
                   CLOB_V(160, 161, 162, 163, 164, 165, 166, 167), CLOB_V(168, 169, 170, 171, 172, 173, 174, 175),
                   CLOB_V(176, 177, 178, 179, 180, 181, 182, 183), CLOB_V(184, 185, 186, 187, 188, 189, 190, 191),
                   CLOB_V(192, 193, 194, 195, 196, 197, 198, 199));
  }

  __builtin_amdgcn_s_setprio(2);
  const int mt64 = 4 * xcd + (idx & 3), nt = idx >> 2;
  const G64 gc = g64_setup(lds0, w, lane);
  const char* wdb = wsp + WS_WD + (size_t)(nt * 112 + 28 * w) * 2048;
  asm volatile("s_barrier" ::: "memory");
  g64_pre(gc, wdb);
#pragma unroll
  for (int bk = 0; bk < 7; ++bk) {
    u32x4 o;
#pragma unroll
    for (int e = 0; e < 4; ++e) {
      const f32x2 gt = {acc[bk][2 * e], acc[bk][2 * e + 1]}, up = {acc[bk][8 + 2 * e], acc[bk][9 + 2 * e]};
      const f32x2 z = gt * __builtin_elementwise_fma(gt * gt, f32x2{-0.10294324f, -0.10294324f}, f32x2{-2.3022082f, -2.3022082f});
      const f32x2 d = f32x2{__builtin_amdgcn_exp2f(z[0]), __builtin_amdgcn_exp2f(z[1])} + f32x2{1.f, 1.f};
      const f32x2 hv = (gt * up) * f32x2{__builtin_amdgcn_rcpf(d[0]), __builtin_amdgcn_rcpf(d[1])};
      o[e] = cvtpk(hv[0], hv[1]);
    }
    store_sc1(wsp + WS_H + (size_t)(mblock * 112 + ft * 7 + bk) * 1024 + lane * 16, o);
  }

  asm volatile("s_waitcnt vmcnt(0)\n\ts_barrier" ::: "memory");
  if (tid == 0) {
    __hip_atomic_fetch_add((unsigned*)(wsp + WS_CNT + mt * 256), 1u, __ATOMIC_RELAXED, __HIP_MEMORY_SCOPE_AGENT);
    const unsigned* cnt = (const unsigned*)(wsp + WS_CNT + (mt64 >> 1) * 256);
    while (__hip_atomic_load(cnt, __ATOMIC_RELAXED, __HIP_MEMORY_SCOPE_AGENT) < 16u) __builtin_amdgcn_s_sleep(4);
  }
  asm volatile("s_barrier" ::: "memory");
  __builtin_amdgcn_s_setprio(0);

  f32x16 d[4];
  const unsigned ao0 = ((mt64 * 2 + (w & 1)) * 112 + 28 * w) * 1024 + lane * 16, ao1 = ((mt64 * 2 + (~w & 1)) * 112 + 28 * w) * 1024 + lane * 16;
  G64_MAIN(G64_DOWN_ASM, d, gc, wdb + 8 * 2048, wsp + WS_H, ao0, ao1);

  __builtin_amdgcn_s_setprio(0);
  const size_t eo = (size_t)(mt64 * 64 + (w & 1) * 32 + (lane & 31)) * 1024 + nt * 128 + (w >> 1) * 64 + (lane >> 5) * 16;
  u32x4 x0, x1;
  asm volatile("global_load_dwordx4 %0, %2, off\n\tglobal_load_dwordx4 %1, %2, off offset:32" : "=&v"(x0), "=&v"(x1) : "v"(x + eo) : "memory");
  float y[16];
  g64_reduce(d, smem, w, lane, y);
  asm volatile("s_waitcnt lgkmcnt(0)\n\ts_barrier" ::: "memory");
  const char* wob = wsp + WS_WO + (size_t)(nt * 32 + 8 * w) * 2048;
  g64_pre(gc, wob);
  const unsigned* ocnt = (const unsigned*)(wsp + WS_CNT + (16 + mt64) * 256);
  unsigned seen;
  asm volatile("global_load_dword %0, %1, %2 sc1" : "=&v"(seen) : "v"(0u), "s"(ocnt) : "memory");
  asm volatile("" : "+v"(y[0]), "+v"(y[1]), "+v"(y[2]), "+v"(y[3]), "+v"(y[4]), "+v"(y[5]), "+v"(y[6]), "+v"(y[7]),
                    "+v"(y[8]), "+v"(y[9]), "+v"(y[10]), "+v"(y[11]), "+v"(y[12]), "+v"(y[13]), "+v"(y[14]), "+v"(y[15]));
  f32x2 gf[8], xf[8];
#pragma unroll
  for (int i = 0; i < 8; ++i) {
    const float y0 = bf_round(y[2 * i]), y1 = bf_round(y[2 * i + 1]);
    const unsigned gv = cvtpk(y0 * __builtin_amdgcn_rcpf(1.f + __builtin_amdgcn_exp2f(y0 * -1.4426950408889634f)),
                              y1 * __builtin_amdgcn_rcpf(1.f + __builtin_amdgcn_exp2f(y1 * -1.4426950408889634f)));
    gf[i] = f32x2{bf_lo(gv), bf_hi(gv)};
  }
  asm volatile("" : "+v"(gf[0]), "+v"(gf[1]), "+v"(gf[2]), "+v"(gf[3]), "+v"(gf[4]), "+v"(gf[5]), "+v"(gf[6]), "+v"(gf[7]) :: "memory");

  unsigned tkeep, tsend, bo, xput, xget;
  {
    const unsigned h = lane >> 5, tr_r = (lane & 15) >> 2, tr_c = lane & 3, sw = (tr_r >> 1) & 1, kh = w >> 1;
    const unsigned base = lds0 + kh * 32768 + (4 * h + tr_r) * 128 + ((lane >> 4) & 1) * 32 + (tr_c & 1) * 16 + (tr_c >> 1) * 8;
    tkeep = base + ((sw ^ kh) << 6);
    tsend = base + ((sw ^ kh ^ 1) << 6);
    bo = ((mt64 * 2 + (w & 1)) * 32 + 16 * kh) * 1024 + lane * 16;
    xput = lds0 + 65536 + lane * 16 + w * 4096;
    xget = lds0 + 65536 + lane * 16 + (w ^ 2) * 4096;
    asm volatile("" : "+{v144}"(tkeep), "+{v145}"(tsend), "+{v146}"(bo), "+{v147}"(xput), "+{v148}"(xget));
  }
  asm volatile("s_waitcnt vmcnt(0)\n\ts_barrier" : "+v"(seen), "+v"(x0), "+v"(x1) :: "memory");
#pragma unroll
  for (int i = 0; i < 8; ++i) {
    const unsigned xv = i < 4 ? x0[i & 3] : x1[i & 3];
    xf[i] = f32x2{bf_lo(xv), bf_hi(xv)};
  }
  asm volatile("" : "+v"(xf[0]), "+v"(xf[1]), "+v"(xf[2]), "+v"(xf[3]), "+v"(xf[4]), "+v"(xf[5]), "+v"(xf[6]), "+v"(xf[7]));
  while (rfl(seen) < 32u) {
    __builtin_amdgcn_s_sleep(1);
    asm volatile("global_load_dword %0, %1, %2 sc1\n\ts_waitcnt vmcnt(0)" : "=&v"(seen) : "v"(0u), "s"(ocnt) : "memory");
  }
  __builtin_amdgcn_s_setprio(2);

  f32x16 keep, recv;
  {
    asm volatile(G64_WO_ASM
                 : "={v[0:15]}"(keep), "={v[16:31]}"(recv)
                 : [ab0] "s"(wsp + WS_O), "{v144}"(tkeep), "{v145}"(tsend), "{v146}"(bo), "{v147}"(xput), "{v148}"(xget)
                 : "memory", "scc", "vcc", "s42", "s43",
                   CLOB_V(32, 33, 34, 35, 36, 37, 38, 39), CLOB_V(40, 41, 42, 43, 44, 45, 46, 47), "v48",
                   CLOB_V(64, 65, 66, 67, 68, 69, 70, 71), CLOB_V(72, 73, 74, 75, 76, 77, 78, 79), CLOB_V(80, 81, 82, 83, 84, 85, 86, 87),
                   CLOB_V(88, 89, 90, 91, 92, 93, 94, 95), CLOB_V(96, 97, 98, 99, 100, 101, 102, 103), CLOB_V(104, 105, 106, 107, 108, 109, 110, 111),
                   CLOB_V(112, 113, 114, 115, 116, 117, 118, 119), CLOB_V(120, 121, 122, 123, 124, 125, 126, 127),
                   CLOB_V(128, 129, 130, 131, 132, 133, 134, 135), CLOB_V(136, 137, 138, 139, 140, 141, 142, 143));
  }

  u32x4 o[2];
#pragma unroll
  for (int i = 0; i < 8; ++i) {
    const f32x2 sv = f32x2{keep[2 * i], keep[2 * i + 1]} + f32x2{recv[2 * i], recv[2 * i + 1]};
    const f32x2 r = __builtin_elementwise_fma(sv, gf[i], xf[i]);
    o[i >> 2][i & 3] = cvtpk(r[0], r[1]);
  }

  char* od = out + eo;
  *(u32x4*)od = o[0];
  *(u32x4*)(od + 32) = o[1];

}

extern "C" __global__ void __launch_bounds__(256, 2)
k2_mix(const __hip_bfloat16* __restrict__ wg, const __hip_bfloat16* __restrict__ wu, const __hip_bfloat16* __restrict__ wd,
       const __hip_bfloat16* __restrict__ x, const __hip_bfloat16* __restrict__ wo, __hip_bfloat16* __restrict__ out,
       char* __restrict__ wsp) {
  extern __shared__ __attribute__((aligned(1024))) char smem[];
  if (blockIdx.x >= 256) {
    ffn_role(blockIdx.x - 256, (const char*)wg, (const char*)wu, (const char*)wd, (const char*)x, (const char*)wo, (char*)out, wsp, smem);
  } else {
    attn_role(blockIdx.x, wsp, (unsigned)(size_t)(LDS(char)*)smem);
  }
}

extern "C" void kernel_launch(void* const* d_in, const int* in_sizes, int n_in,
                              void* d_out, int out_size, void* d_ws, size_t ws_size,
                              hipStream_t stream) {
  const __hip_bfloat16* x = (const __hip_bfloat16*)d_in[0];
  const __hip_bfloat16* wqkv = (const __hip_bfloat16*)d_in[1];
  const __hip_bfloat16* wo = (const __hip_bfloat16*)d_in[2];
  const __hip_bfloat16* wg = (const __hip_bfloat16*)d_in[4];
  const __hip_bfloat16* wu = (const __hip_bfloat16*)d_in[5];
  const __hip_bfloat16* wd = (const __hip_bfloat16*)d_in[6];
  char* ws = (char*)d_ws;
  hipFuncSetAttribute((const void*)k1_qkv, hipFuncAttributeMaxDynamicSharedMemorySize, K1_LDS);
  hipFuncSetAttribute((const void*)k2_mix, hipFuncAttributeMaxDynamicSharedMemorySize, K2_LDS);
  k1_qkv<<<256, 512, K1_LDS, stream>>>(x, wqkv, wg, wu, wd, wo, ws);
  k2_mix<<<512, 256, K2_LDS, stream>>>(wg, wu, wd, x, wo, (__hip_bfloat16*)d_out, ws);
}
```
